```python
import torch

def _softmax(x, axis=-1):
    return torch.softmax(x, dim=axis)

def _gelu(x):
    return torch.nn.functional.gelu(x, approximate='tanh')

def _silu(x):
    return torch.nn.functional.silu(x)

def _sigmoid(x):
    return torch.sigmoid(x)

def _layernorm(x, axis=-1, eps=1e-05):
    m = x.mean(dim=axis, keepdim=True)
    v = x.var(dim=axis, keepdim=True, unbiased=False)
    return (x - m) / torch.sqrt(v + eps)

def _scatter_add(tgt, idx, src, axis=0):
    return tgt.clone().index_add_(axis, idx, src)

def _segment_sum(src, seg, n):
    out = torch.zeros((n,) + src.shape[1:], dtype=src.dtype)
    return out.index_add_(0, seg, src)

def _pad(x, pw):
    flat = tuple((v for pair in reversed(pw) for v in pair))
    return torch.nn.functional.pad(x, flat)
kernel_name = 'interleave_layers__memory_aug_recurrent_mem_tr__glu_gated_geglu__prompt'

def setup_inputs():

    _w = lambda a: (lambda t: t.to(torch.bfloat16) if t.is_floating_point() else t)(torch.as_tensor(a))

    def _A_setup():
        B, S, Dm = (1, 2048, 576)
        H, Dh = (32, 128)
        M, seg = (8, 32)
        seg = min(seg, S)
        S = S // seg * seg
        return {'x': _w((lambda *s: torch.randn(*s, dtype=torch.bfloat16) * 0.05)(B, S, Dm)), 'mem0': _w((lambda *s: torch.randn(*s, dtype=torch.bfloat16) * 0.2)(B, M, Dm)), 'Wq': _w((lambda *s: torch.randn(*s, dtype=torch.bfloat16) * 1.6)(Dm, H, Dh) * Dm ** (-0.5)), 'Wk': _w((lambda *s: torch.randn(*s, dtype=torch.bfloat16) * 1.6)(Dm, H, Dh) * Dm ** (-0.5)), 'Wv': _w((lambda *s: torch.randn(*s, dtype=torch.bfloat16) * 0.2)(Dm, H, Dh) * Dm ** (-0.5)), 'Wo': _w((lambda *s: torch.randn(*s, dtype=torch.bfloat16) * 0.2)(H, Dh, Dm) * (H * Dh) ** (-0.5)), 'seg_len': seg}
    _A_in = _A_setup()

    def _B_setup():
        B, S, Dm, Df = (1, 2048, 576, 4608)
        return {'x': _w((lambda *s: torch.randn(*s, dtype=torch.bfloat16) * 0.05)(B, S, Dm)), 'w_gate': _w((lambda *s: torch.randn(*s, dtype=torch.bfloat16) * 0.4)(Dm, Df) * Dm ** (-0.5)), 'w_up': _w((lambda *s: torch.randn(*s, dtype=torch.bfloat16) * 0.4)(Dm, Df) * Dm ** (-0.5)), 'w_down': _w((lambda *s: torch.randn(*s, dtype=torch.bfloat16) * 0.4)(Df, Dm) * Df ** (-0.5))}
    _B_in = _B_setup()
    return {'A_x': _A_in['x'], 'A_mem0': _A_in['mem0'], 'A_Wq': _A_in['Wq'], 'A_Wk': _A_in['Wk'], 'A_Wv': _A_in['Wv'], 'A_Wo': _A_in['Wo'], 'A_seg_len': _A_in['seg_len'], 'B_x': _B_in['x'], 'B_w_gate': _B_in['w_gate'], 'B_w_up': _B_in['w_up'], 'B_w_down': _B_in['w_down']}

def reference(A_x, A_mem0, A_Wq, A_Wk, A_Wv, A_Wo, A_seg_len, B_x, B_w_gate, B_w_up, B_w_down):

    def _A_ref(x, mem0, Wq, Wk, Wv, Wo, seg_len):
        B, S, Dm = x.shape
        H, Dh = (Wq.shape[1], Wq.shape[2])
        M = mem0.shape[1]
        scale = Dh ** (-0.5)
        n_seg = S // seg_len
        L = M + seg_len
        idx = torch.arange(L)
        mask = (idx[:, None] < M) | (idx[:, None] >= idx[None, :])
        mem = mem0
        outs = []
        for s in range(n_seg):
            xs = x[:, s * seg_len:(s + 1) * seg_len, :].clone()
            h = torch.concatenate([mem, xs], axis=1)
            q = torch.einsum('btd,dhe->bhte', h, Wq)
            k = torch.einsum('btd,dhe->bhte', h, Wk)
            v = torch.einsum('btd,dhe->bhte', h, Wv)
            scores = torch.einsum('bhqe,bhke->bhqk', q, k) * scale
            scores = torch.where(mask[None, None], scores, -1000000000.0)
            p = _softmax(scores, axis=-1)
            o = torch.einsum('bhqk,bhke->bhqe', p, v)
            o = torch.einsum('bhte,hed->btd', o, Wo)
            mem = o[:, :M, :].clone()
            outs.append(o[:, M:, :])
        return torch.concatenate(outs, axis=1)

    def _B_ref(x, w_gate, w_up, w_down):
        g = torch.einsum('bsd,df->bsf', x, w_gate)
        u = torch.einsum('bsd,df->bsf', x, w_up)
        h = _gelu(g) * u
        return torch.einsum('bsf,fd->bsd', h, w_down)
    x = A_x
    x = x + _A_ref(_layernorm(x, axis=-1), A_mem0, A_Wq, A_Wk, A_Wv, A_Wo, A_seg_len)
    x = x + _B_ref(_layernorm(x, axis=-1), B_w_gate, B_w_up, B_w_down)
    x = x + _A_ref(_layernorm(x, axis=-1), A_mem0, A_Wq, A_Wk, A_Wv, A_Wo, A_seg_len)
    x = x + _B_ref(_layernorm(x, axis=-1), B_w_gate, B_w_up, B_w_down)
    return x
```

```cpp
#include <hip/hip_runtime.h>
#include <stdint.h>

typedef unsigned v2u __attribute__((ext_vector_type(2)));
typedef unsigned v4u __attribute__((ext_vector_type(4)));
typedef unsigned v8u __attribute__((ext_vector_type(8)));
typedef float f2 __attribute__((ext_vector_type(2)));
typedef float f4 __attribute__((ext_vector_type(4)));
typedef short s4 __attribute__((ext_vector_type(4)));
typedef __bf16 bf2 __attribute__((ext_vector_type(2)));
typedef __bf16 bf8 __attribute__((ext_vector_type(8)));
typedef __amdgpu_buffer_rsrc_t rsrc_t;
#define LAS __attribute__((address_space(3)))
#define GAS __attribute__((address_space(1)))
#define DI __device__ __forceinline__
#define SB() __builtin_amdgcn_sched_barrier(0)
#define BAR() do { __builtin_amdgcn_s_barrier(); asm volatile("" ::: "memory"); } while (0)
#define WAITL() asm volatile("s_waitcnt lgkmcnt(0)" ::: "memory")
#define WAITV(n) asm volatile("s_waitcnt vmcnt(" #n ")" ::: "memory")

#define PLACE 4
#define QDIE 0u
#define TABS 16u
#define XBDIE 1u
#define ODIE 1u
#define HDIE 1u
#define FDIE 1u
#define PSLEEP 2
#define NPROBE 4
#define DLY_GU 0
#define DLY_WO 0
#define DLY_DN 0
#define DLY_CH 300
#define D3V 30
#define D4V 20
#define D1V 2000
#define D2V 58

constexpr int DM = 576, NSEG = 64, DF = 4608;
constexpr unsigned XBYTES = 2048u * 576u * 2u;
constexpr unsigned OFF_CTL = 131072;
constexpr unsigned CLEAR_BYTES = OFF_CTL + 1024u;
constexpr unsigned RQ = 16, RK = 8, RB = 8;
constexpr unsigned HREG = (RQ * 49152u + RK * 16384u + 0xFFFFFu) & ~0xFFFFFu;
constexpr unsigned PG = 2097152u;
constexpr unsigned OFF_MSG = PG;
constexpr unsigned OFF_WOS = 3 * PG;
constexpr unsigned OFF_X = 6 * PG;
enum { X_A0 = 0, X_A1, X_B0, X_B1, X_R0, X_R1, X_R2 };
constexpr unsigned OFF_O = OFF_X + 28 * PG;
constexpr unsigned OFF_HB = OFF_O + 2 * PG;
constexpr unsigned OFF_PW = OFF_HB + 4 * PG;
constexpr unsigned OFF_PD = OFF_PW + 2 * PG;
constexpr unsigned LSTR = 10 * PG;
constexpr unsigned OFF_HD = OFF_O + 2 * LSTR;
constexpr unsigned WS_USED = OFF_HD + 64u * HREG;
static_assert(RB == 8 && WS_USED <= (256u << 20), "");
constexpr unsigned OOB = 0x80000000u;

constexpr int N_CH = 0, N_QKV = 64, N_AT = 128, N_WO = 144, N_GU = 168, N_DN = 216, N_RD = 240;
constexpr int LDS_BYTES = 163840;
constexpr float SM_C = 0.08838834764831845f * 1.4426950408889634f;

struct Params {
  const char *x, *mem0, *wq, *wk, *wv, *wo, *wg, *wu, *wd;
  char *out, *ws;
};
#define TV(i, dflt) (dflt)

template <int N> struct IC { static constexpr int v = N; };
template <int B, int E, class F> DI void sfor(F&& f) { if constexpr (B < E) { f(IC<B>{}); sfor<B + 1, E>(f); } }

DI unsigned long long rtclk() { unsigned long long t; asm volatile("s_memrealtime %0\n\ts_waitcnt lgkmcnt(0)" : "=s"(t) :: "memory"); return t; }
DI unsigned cyclk() { unsigned long long t; asm volatile("s_memtime %0\n\ts_waitcnt lgkmcnt(0)" : "=s"(t) :: "memory"); return (unsigned)t; }
DI int rfl(int v) { return __builtin_amdgcn_readfirstlane(v); }
template <int N, class V> DI void uni(V& v) {
  #pragma unroll
  for (int i = 0; i < N; ++i) v[i] = (unsigned)rfl((int)v[i]); }
DI unsigned pk(float a, float b) { f2 v = {a, b}; bf2 r = __builtin_convertvector(v, bf2); return __builtin_bit_cast(unsigned, r); }
DI float blo(unsigned u) { return __builtin_bit_cast(float, u << 16); }
DI float bhi(unsigned u) { return __builtin_bit_cast(float, u & 0xFFFF0000u); }
DI float rb(float x) { return blo(pk(x, x)); }
DI rsrc_t mk(const void* p, unsigned n) { return __builtin_amdgcn_make_buffer_rsrc((void*)p, 0, n, 0x00020000); }
DI v4u ld4(rsrc_t r, unsigned v, unsigned s) { return __builtin_amdgcn_raw_buffer_load_b128(r, v, s, 16); }
DI v2u ld2(rsrc_t r, unsigned v, unsigned s) { return __builtin_amdgcn_raw_buffer_load_b64(r, v, s, 16); }
DI void st4(v4u d, rsrc_t r, unsigned v, unsigned s) { __builtin_amdgcn_raw_buffer_store_b128(d, r, v, s, 16); asm volatile("s_nop 1" :: "v"(d) : "memory"); }
DI int ld1(rsrc_t r, unsigned v, unsigned s) { return (int)__builtin_amdgcn_raw_buffer_load_b32(r, v, s, 16); }
DI void st2(v2u d, rsrc_t r, unsigned v, unsigned s) { __builtin_amdgcn_raw_buffer_store_b64(d, r, v, s, 16); }
DI void st1(unsigned d, rsrc_t r, unsigned v, unsigned s) { __builtin_amdgcn_raw_buffer_store_b32(d, r, v, s, 16); }
DI void dma(rsrc_t r, unsigned v, unsigned s, LAS void* l) { __builtin_amdgcn_raw_ptr_buffer_load_lds(r, l, 16, v, s, 0, 16); }
DI void dmaw(rsrc_t r, unsigned v, unsigned s, LAS void* l) { __builtin_amdgcn_raw_ptr_buffer_load_lds(r, l, 16, v, s, 0, 0); }
DI int min4(v4u v) { return min(min((int)v.x, (int)v.y), min((int)v.z, (int)v.w)); }
DI void nops(int n) { asm volatile("" : "+s"(n)); for (int i = 0; i < n; ++i) asm volatile("s_nop 0"); }
template <int O> DI void ldq(v4u& d, unsigned voff, const char* base) { asm volatile("global_load_dwordx4 %0, %1, %2 offset:%3 sc1" : "=v"(d) : "v"(voff), "s"(base), "i"(O) : "memory"); }

DI void ldq1(int& d, unsigned voff, const char* base) { asm volatile("global_load_dword %0, %1, %2 sc1" : "=v"(d) : "v"(voff), "s"(base) : "memory"); }
template <int O> DI void ldp(v4u& d, unsigned voff, const char* base) { asm volatile("global_load_dwordx4 %0, %1, %2 offset:%3" : "=v"(d) : "v"(voff), "s"(base), "i"(O) : "memory"); }
template <int N> DI void waiti(int& a) { asm volatile("s_waitcnt vmcnt(%1)" : "+v"(a) : "i"(N) : "memory"); }
template <int N> DI void waitv1(v4u& a) { asm volatile("s_waitcnt vmcnt(%1)" : "+v"(a) : "i"(N) : "memory"); }

DI int lane_id() { int ln; asm volatile("v_mbcnt_lo_u32_b32 %0, -1, 0\n\tv_mbcnt_hi_u32_b32 %0, -1, %0" : "=v"(ln)); return ln; }
struct Ctx {
  rsrc_t ws; const char* wsp; unsigned long long deadline; int lane, w;
  unsigned par;
};
DI unsigned near(const Ctx& c, unsigned off, unsigned die) { return off + (((c.par ^ die ^ (off >> 21)) & 1u) << 13); }
DI unsigned DIE_CH(int l) { return (PLACE == 8 || PLACE == 1) ? (unsigned)l : 0u; }
DI unsigned flagoff(const Ctx& c, int node, int slot) { unsigned i = (unsigned)(node * 2 + slot); return near(c, (i >> 6) * 16384u + (i & 63u) * 128u, node < N_QKV ? DIE_CH(node >> 5) : (node < N_AT ? DIE_CH(slot) : ((PLACE == 8 || PLACE == 1) ? (unsigned)((node < N_WO || node >= N_RD) ? (node >> 3) & 1 : slot) : FDIE))); }
DI unsigned head_base(const Ctx& c, int l, int h) { return near(c, OFF_HD + (unsigned)(l * 32 + h) * HREG, DIE_CH(l) ^ QDIE); }
DI unsigned xrow(const Ctx& c, int b, int r) { return near(c, OFF_X + (unsigned)b * (4 * PG) + (unsigned)(r >> 2) * 1048576u, (b == X_B0 || b == X_B1) ? XBDIE : 1u) + (unsigned)(((r >> 2) & 1) * 3584 + (r & 3) * 1152); }
DI unsigned kvm_base(const Ctx& c, int l, int h) { return near(c, OFF_HD + (unsigned)(l * 32 + h) * HREG, DIE_CH(l)) + RQ * 49152u; }
DI bool late(const Ctx& c) { return rtclk() > c.deadline; }
DI void wait_flags(const Ctx& c, unsigned voff, int s) {
  for (;;) {
    asm volatile("" ::: "memory");
    if (__all(ld1(c.ws, voff, 0) >= s)) break;
    if (late(c)) break;
    __builtin_amdgcn_s_sleep(PSLEEP);
  }
  asm volatile("" ::: "memory");
}
#define STAMPT(node, i)
#define CYC(x)
#define GET(x)
#define ACC(i, a, b)
DI void get_par(Ctx& c) {
  for (;;) { asm volatile("" ::: "memory"); int v = rfl(ld1(c.ws, 0u, OFF_CTL)); c.par = (unsigned)v & 1u; if (v >= 0 || late(c)) break; __builtin_amdgcn_s_sleep(2); }
}
DI void publish(const Ctx& c, int node, int slot, int s) {
  st1((unsigned)s, c.ws, threadIdx.x == 0 ? 0u : OOB, flagoff(c, node, slot));
}

template <bool WA, bool SWAP, bool ZERO> DI void mm(f4& acc, const v4u& w, const v4u& x) {
  if constexpr (ZERO) {
    if constexpr (WA) { if constexpr (SWAP) asm volatile("v_mfma_f32_16x16x32_bf16 %0, %2, %1, 0" : "=&v"(acc) : "a"(w), "v"(x)); else asm volatile("v_mfma_f32_16x16x32_bf16 %0, %1, %2, 0" : "=&v"(acc) : "a"(w), "v"(x)); }
    else { if constexpr (SWAP) asm volatile("v_mfma_f32_16x16x32_bf16 %0, %2, %1, 0" : "=&v"(acc) : "v"(w), "v"(x)); else asm volatile("v_mfma_f32_16x16x32_bf16 %0, %1, %2, 0" : "=&v"(acc) : "v"(w), "v"(x)); }
  } else {
    if constexpr (WA) { if constexpr (SWAP) asm volatile("v_mfma_f32_16x16x32_bf16 %0, %2, %1, %0" : "+v"(acc) : "a"(w), "v"(x)); else asm volatile("v_mfma_f32_16x16x32_bf16 %0, %1, %2, %0" : "+v"(acc) : "a"(w), "v"(x)); }
    else { if constexpr (SWAP) asm volatile("v_mfma_f32_16x16x32_bf16 %0, %2, %1, %0" : "+v"(acc) : "v"(w), "v"(x)); else asm volatile("v_mfma_f32_16x16x32_bf16 %0, %1, %2, %0" : "+v"(acc) : "v"(w), "v"(x)); }
  }
}
DI void mmz(f4& acc, const v4u& a, const v4u& b) { asm volatile("s_nop 1\n\tv_mfma_f32_16x16x32_bf16 %0, %1, %2, 0" : "=&v"(acc) : "v"(a), "v"(b)); }
DI void mma(f4& acc, const v4u& a, const v4u& b) { asm volatile("s_nop 1\n\tv_mfma_f32_16x16x32_bf16 %0, %1, %2, %0" : "+v"(acc) : "v"(a), "v"(b)); }
DI void mma16(f4& acc, const v2u& a, const v2u& b) { asm volatile("s_nop 1\n\tv_mfma_f32_16x16x16_bf16 %0, %1, %2, %0" : "+v"(acc) : "v"(a), "v"(b)); }
template <bool WA> DI void sm(f4& acc, const v4u& a, const v8u& b, unsigned idx) {
  if constexpr (WA) asm volatile("v_smfmac_f32_16x16x64_bf16 %0, %1, %2, %3" : "+v"(acc) : "v"(a), "a"(b), "v"(idx));
  else asm volatile("v_smfmac_f32_16x16x64_bf16 %0, %1, %2, %3" : "+v"(acc) : "v"(a), "v"(b), "v"(idx));
}
DI void settle(f4& a) { asm volatile("s_nop 7" : "+v"(a)); }
DI void settle2(f4& a, f4& b) { asm volatile("s_nop 7" : "+v"(a), "+v"(b)); }
DI void settle3(f4& a, f4& b, f4& c) { asm volatile("s_nop 7" : "+v"(a), "+v"(b), "+v"(c)); }
DI void tie(f4& a) { asm volatile("" : "+v"(a)); }

DI float fmx(float a, float b) { return __builtin_elementwise_maximum(a, b); }
DI float x8(float v) { return __builtin_bit_cast(float, __builtin_amdgcn_update_dpp(0, __builtin_bit_cast(int, v), 0x128, 0xF, 0xF, true)); }
template <int CTRL> DI float dpp(float v) { return __builtin_bit_cast(float, __builtin_amdgcn_update_dpp(0, __builtin_bit_cast(int, v), CTRL, 0xF, 0xF, true)); }
DI float max16(float v) { unsigned u = __builtin_bit_cast(unsigned, v); auto r = __builtin_amdgcn_permlane16_swap(u, u, false, false); return fmx(__builtin_bit_cast(float, (unsigned)r[0]), __builtin_bit_cast(float, (unsigned)r[1])); }
DI float max32(float v) { unsigned u = __builtin_bit_cast(unsigned, v); auto r = __builtin_amdgcn_permlane32_swap(u, u, false, false); return fmx(__builtin_bit_cast(float, (unsigned)r[0]), __builtin_bit_cast(float, (unsigned)r[1])); }
DI float add16(float v) { unsigned u = __builtin_bit_cast(unsigned, v); auto r = __builtin_amdgcn_permlane16_swap(u, u, false, false); return __builtin_bit_cast(float, (unsigned)r[0]) + __builtin_bit_cast(float, (unsigned)r[1]); }
DI float add32(float v) { unsigned u = __builtin_bit_cast(unsigned, v); auto r = __builtin_amdgcn_permlane32_swap(u, u, false, false); return __builtin_bit_cast(float, (unsigned)r[0]) + __builtin_bit_cast(float, (unsigned)r[1]); }
DI float fold32(float a, float b) { auto r = __builtin_amdgcn_permlane32_swap(__builtin_bit_cast(unsigned, a), __builtin_bit_cast(unsigned, b), false, false); return __builtin_bit_cast(float, (unsigned)r[0]) + __builtin_bit_cast(float, (unsigned)r[1]); }
DI float rsum(float v) {
  v += dpp<0xB1>(v); v += dpp<0x4E>(v); v += dpp<0x124>(v); v += dpp<0x128>(v);
  return add32(v);
}

DI int piece_k(int lane) { int rs = lane >> 1; return (rs & 0x13) | ((rs & 8) >> 1) | ((rs & 4) << 1); }
DI unsigned tr_off(int lane) { int g = lane >> 4, q = (lane >> 2) & 3, pp = lane & 3; return (unsigned)((16 * (g >> 1) + 4 * (g & 1) + q) * 32 + pp * 8); }
DI v4u tr_frag(LAS char* p) {
  v2u a = __builtin_bit_cast(v2u, __builtin_amdgcn_ds_read_tr16_b64_v4i16((LAS s4*)p));
  v2u b = __builtin_bit_cast(v2u, __builtin_amdgcn_ds_read_tr16_b64_v4i16((LAS s4*)(p + 256)));
  return (v4u){a.x, a.y, b.x, b.y};
}
DI unsigned pair_chunk(int lane) { return (unsigned)(((lane & 3) ^ ((lane >> 4) & 2)) * 16); }
DI unsigned tr_off2(int lane) { int g = lane >> 4, q = (lane >> 2) & 3, pp = lane & 3; return (unsigned)((8 * g + q) * 64 + (((pp >> 1) ^ ((g & 1) << 1)) * 16) + (pp & 1) * 8); }
template <int N, int CH, class KIND, class SRC, class DST> DI void load_frags(LAS char* stage, int lane, KIND&& kind, SRC&& src, DST&& dst) {
  constexpr int R = N / CH;
  static_assert(N % CH == 0, "");
  LAS char* rd1 = stage + tr_off(lane);
  LAS char* rdA = stage + tr_off2(lane);
  LAS char* rdB = stage + (tr_off2(lane) ^ 32u);
  sfor<0, CH>([&](auto i) { src(IC<decltype(i)::v>{}, stage + decltype(i)::v * 1024); });
  sfor<0, R>([&](auto rc) { constexpr int r = decltype(rc)::v;
    if constexpr (r + 1 < R) {
      WAITL();
      sfor<0, CH>([&](auto i) { src(IC<(r + 1) * CH + decltype(i)::v>{}, stage + (((r + 1) & 1) * CH + decltype(i)::v) * 1024); });
      asm volatile("s_waitcnt vmcnt(%0)" :: "i"(CH) : "memory");
    } else { WAITV(0); }
    sfor<0, CH>([&](auto i) { constexpr int n = r * CH + decltype(i)::v, sl = (r & 1) * CH + decltype(i)::v; constexpr int kd = decltype(kind(IC<n>{}))::v;
      dst(IC<n>{}, tr_frag(kd == 2 ? rd1 + sl * 1024 : (kd == 0 ? rdA + sl * 1024 : rdB + (sl - 1) * 1024))); });
    SB();
  });
  WAITL();
}

DI void warm(const Params& p, LAS char* L, int lane, int w, int x, int id, int nw, int nw2, bool with_wo) {
  rsrc_t rq = mk(p.wq, 576u * 8192u), rk = mk(p.wk, 576u * 8192u), rv = mk(p.wv, 576u * 8192u), ro = mk(p.wo, 4096u * 1152u);
  const int total = with_wo ? 4608 : 3456;
  int n = 0;
  for (int c = id; c < total; c += nw) {
    int m, i;
    if (with_wo) { m = c / 1152; i = c - m * 1152; } else { i = c / 3; m = c - 3 * i; m = m == 2 ? 3 : m; }
    unsigned o = m == 2 ? (unsigned)(x * 1024) * 1152u + (unsigned)i * 1024u : (unsigned)(i >> 1) * 8192u + (unsigned)(x * 2048 + (i & 1) * 1024);
    LAS char* d = L + w * 1024;
    if (m == 0) dmaw(rq, lane * 16, o, d); else if (m == 1) dmaw(rv, lane * 16, o, d); else if (m == 2) dmaw(ro, lane * 16, o, d); else dmaw(rk, lane * 16, o, d);
    if (++n >= 40) WAITV(39);
  }
  rq = mk(with_wo ? p.wg : p.wo, with_wo ? 576u * 9216u : 4096u * 1152u); rk = mk(with_wo ? p.wu : p.wd, with_wo ? 576u * 9216u : 4608u * 1152u);
  for (int c = id < nw2 ? id : 4096; c < (with_wo ? 2592 : 2448); c += nw2) {
    LAS char* d = L + w * 1024;
    if (with_wo) { unsigned q = (unsigned)(c >> 1) * 64u + (unsigned)lane, row = q / 144u, vo = row * 9216u + (q - row * 144u) * 16u;
      if (c & 1) dmaw(rk, vo, x * 2304, d); else dmaw(rq, vo, x * 2304, d); }
    else if (c < 1152) dmaw(rq, lane * 16, (unsigned)(x * 1152 + c) * 1024u, d);
    else dmaw(rk, lane * 16, (unsigned)(x * 1296 + c - 1152) * 1024u, d);
    WAITV(39);
  }
  WAITV(0); BAR();
}

typedef unsigned v16u __attribute__((ext_vector_type(16)));
typedef unsigned v32u __attribute__((ext_vector_type(32)));
template <int O, class V> DI void put4(V& d, v4u f) { d[O] = f.x; d[O + 1] = f.y; d[O + 2] = f.z; d[O + 3] = f.w; }
enum { M_QKV = 0, M_GU = 1, M_WO = 2, M_DN = 3 };
struct GemmCfg {
  unsigned in0, in1, in_stride;
  unsigned out0, out1, out_stride;
  int node, alt;
  unsigned flaga;
  unsigned flagv;
  unsigned ov0, ov1;
  unsigned os1, os2;
};
struct GemmW { v32u a[13]; v16u b; v8u c; };
template <int N, int NF> DI void putw(GemmW& W, v4u f) { if constexpr (N < (NF & ~7)) put4<(N & 7) * 4>(W.a[N >> 3], f); else if constexpr (N < (NF & ~3)) put4<(N & 3) * 4>(W.b, f); else put4<(N & 1) * 4>(W.c, f); }

template <int MODE> DI void gemm_loop(const Ctx& c, const GemmCfg& g, LAS char* L, GemmW& W) {
  constexpr int KS = MODE == M_WO ? 16 : 18, JB = KS * 2048;
  constexpr bool ROWM = MODE < M_WO;
  const int lane = c.lane, wv = c.w;
  const unsigned lb = (unsigned)(size_t)L;
  v8u c0; v4u c1, s0; v16u s1;
  unsigned rb;
  if constexpr (ROWM) {
    int r8 = lane >> 3, c8 = lane & 7; c0[0] = xrow(c, MODE == M_QKV ? X_A0 : X_B0, 8 * wv + r8) + (unsigned)((c8 ^ (r8 & 6)) * 16);
    int tok = lane & 15, gg = lane >> 4; rb = (unsigned)((tok >> 3) * 9216 + (tok & 7) * 128 + ((gg ^ (tok & 6)) * 16));
  } else { c0[0] = (unsigned)((wv >> 1) * 16384 + (wv & 1) * 4096 + lane * 16); rb = (unsigned)lane * 16u; }
  c0[1] = lb + rb; c0[2] = lb + (rb ^ 64u); c0[3] = g.flagv; c0[4] = g.ov0; c0[5] = g.ov1;
  c0[6] = lb + 2 * JB + 1024 + wv * 2048 + (lane & 15) * 112 + (lane >> 4) * 8;
  c0[7] = lb + 2 * JB + 1024 + wv * 2048 + (lane / 6) * 112 + (lane % 6) * 16;
  if constexpr (MODE == M_GU) {
    int r8 = lane >> 3; unsigned cb = (unsigned)((wv >> 1) * 512 + (((lane & 7) ^ (r8 & 6)) * 16));
    c0[0] = xrow(c, X_B0, 8 * (wv & 1) + r8) + cb; c0[6] = xrow(c, X_B0, 16 + 8 * (wv & 1) + r8) + cb; }
  c1[0] = c0[1] + JB; c1[1] = ROWM ? c0[2] + JB : (unsigned)(wv * 1024 + lane * 16); c1[2] = lb + 2 * JB; c1[3] = g.flaga;
  unsigned long long wp = (unsigned long long)c.wsp;
  s0[0] = (unsigned)wp; s0[1] = (unsigned)(wp >> 32) & 0xFFFFu; s0[2] = WS_USED; s0[3] = 0x00020000u;
  s1[0] = (unsigned)wp; s1[1] = (unsigned)(wp >> 32); s1[2] = g.in0; s1[3] = g.in1; s1[4] = g.out0; s1[5] = g.out1; s1[6] = g.in_stride; s1[7] = g.out_stride;
  s1[8] = flagoff(c, g.node, 0); s1[9] = (unsigned)g.node; s1[10] = g.os1; s1[11] = g.os2; s1[12] = (unsigned)c.deadline; s1[13] = (unsigned)wv;
  s1[14] = lb + (MODE == M_GU ? (unsigned)((wv & 1) * 9216 + (wv >> 1) * 4096) : (unsigned)wv * (ROWM ? 9216u : 4096u)); s1[15] = lb + (unsigned)wv * 1024u;
  uni<16>(s1);
  const int fd = rfl((int)(flagoff(c, g.node, 1) - flagoff(c, g.node, 0)));
  STAMPT(g.node, 128);
#define GEMM_OUT "+{s[52:67]}"(s1)
#define GEMM_IN "{v[0:7]}"(c0), "{v[8:11]}"(c1), "{s[36:39]}"(s0), "{s94}"(fd), "{a[0:31]}"(W.a[0]), "{a[32:63]}"(W.a[1]), "{a[64:95]}"(W.a[2]), "{a[96:127]}"(W.a[3]), "{a[128:159]}"(W.a[4]), "{a[160:191]}"(W.a[5])
#define GEMM_CLOBBERS "v12", "v16", "v17", "v18", "v19", "v20", "v21", "v22", "v23", "v24", "v25", "v26", "v27", "v28", "v29", "v30", "v31", "v32", "v33", "v34", \
  "v35", "v36", "v37", "v38", "v39", "v40", "v41", "v42", "v43", "v44", "v45", "v46", "v47", "v48", "v49", "v50", "v51", "v52", "v53", "v54", \
  "v55", "v56", "v57", "v58", "v59", "v60", "v61", "v62", "v63", "v64", "v65", "v66", "v67", "v68", "v69", "v70", "v71", "v72", "v73", "v74", \
  "v75", "v76", "v77", "v78", "v79", "v80", "v81", "v82", "v83", "v84", "v85", "v86", "v87", "v88", "v89", "v90", "v91", "v92", "v93", "v94", \
  "v95", "v96", "v97", "v98", "v99", "v100", "v101", "v102", "v103", "v104", "v105", "v106", "v107", "v108", "v109", "v110", "v111", "v112", "v113", "v114", \
  "v115", "v116", "v117", "v118", "v119", "v120", "v121", "v122", "v123", "v124", "v125", "v126", "v127", "v128", "v129", "v130", "v131", "v132", "v133", "v134", \
  "v135", "v136", "v137", "v138", "v139", "v140", "v141", "v142", "v143", "v144", "v145", "v146", "v147", "v148", "v149", "v150", "v151", "v152", "v153", "v154", \
  "v155", "v156", "v157", "v158", "v159", "v160", "v161", "v162", "v163", "v164", "v165", "v166", "v167", "v168", "v169", "v170", "v171", "v172", "v173", "v174", \
  "v175", "v176", "v177", "v178", "v179", "v180", "v181", "v182", "v183", "v184", "v185", "v186", "v187", "v188", "v189", "v190", "v191", "v192", "v193", "v194", \
  "v195", "v196", "v197", "v198", "v199", "v200", "v201", "v202", "v203", "v204", "v205", "v206", "v207", "v208", "v209", "v210", "v211", "v212", "v213", "v214", \
  "v215", "v216", "v217", "v218", "v219", "v220", "v221", "v222", "v223", "v224", "v225", "v226", "v227", "v228", "v229", "v230", "v231", "v232", "v233", "v234", \
  "v235", "v236", "v237", "v238", "v239", "v240", "v241", "v242", "v243", "v244", "v245", "v246", "v247", "v248", "v249", "v250", "v251", "v252", "v253", "v254", \
  "v255", "s40", "s41", "s42", "s43", "s44", "s45", "s46", "s47", "s48", "s49", "s50", "s51", "s68", "s69", "s70", "s71", "s72", "s73", "s74", \
  "s75", "s76", "s77", "s78", "s79", "s80", "s81", "s82", "s83", "s84", "s85", "s86", "s87", "s88", "s89", "s90", "s91", "s92", "s93", "vcc", \
  "scc", "memory"
#define PART_CLOBBERS "v12", "v16", "v17", "v18", "v19", "v20", "v21", "v22", "v23", "v24", "v25", "v26", "v27", "v28", "v29", "v30", "v31", "v32", "v33", "v34", \
  "v35", "v36", "v37", "v38", "v39", "v40", "v41", "v42", "v43", "v44", "v45", "v46", "v47", "v48", "v49", "v50", "v51", "v52", "v53", "v54", \
  "v55", "v56", "v57", "v58", "v59", "v60", "v61", "v62", "v63", "v64", "v65", "v66", "v67", "s40", "s41", "s42", "s43", "s44", "s45", "s46", \
  "s47", "s48", "s49", "s50", "s51", "s68", "s69", "s70", "s71", "s72", "s73", "s74", "s75", "s76", "s77", "s78", "s79", "s80", "s81", "s82", \
  "s83", "s84", "s85", "s86", "s87", "s88", "s89", "s90", "s91", "s92", "s93", "vcc", "scc", "memory"
#define GEMM_IN2 "{a[192:207]}"(W.b), "{a[208:215]}"(W.c)
#define GEMM_IN3 "{a[192:223]}"(W.a[6]), "{a[224:255]}"(W.a[7]), "{v[80:111]}"(W.a[8]), "{v[112:143]}"(W.a[9]), "{v[144:175]}"(W.a[10]), "{v[176:207]}"(W.a[11])
  if constexpr (MODE == M_QKV) asm volatile(
    "s_nop 4\n"
    "s_mov_b32 s40, s36\n"
    "s_mov_b32 s41, s37\n"
    "s_mov_b32 s42, 0\n"
    "s_mov_b32 s43, s39\n"
    "s_mov_b32 s44, s36\n"
    "s_mov_b32 s45, s37\n"
    "s_mov_b32 s46, 0\n"
    "s_mov_b32 s47, s39\n"
    "s_mov_b32 s68, 0\n"
    "s_mov_b32 s69, 0\n"
    "s_mov_b32 s70, 0\n"
    "s_mov_b32 s72, 0\n"
    "s_mov_b32 s81, 0\n"
    "s_mov_b32 s82, 0\n"
    "s_mov_b32 s83, 0\n"
    "s_mov_b32 s91, 0\n"
    "s_lshr_b32 s90, 1, s65\n"
    "s_lshl_b32 s73, s65, 2\n"
    "v_add_u32 v12, s73, v10\n"
    "v_mov_b32 v18, 0xbdd2d3e8\n"
    "s_branch L_start%=\n"
    "L_body0%=:\n"
    "s_waitcnt lgkmcnt(0)\n"
    "v_mfma_f32_16x16x32_bf16 v[36:39], a[0:3], v[20:23], 0\n"
    "ds_read_b128 v[32:35], v2 offset:18432\n"
    "v_mov_b32 v19, s83\n"
    "v_mfma_f32_16x16x32_bf16 v[40:43], a[0:3], v[24:27], 0\n"
    "ds_read_b128 v[28:31], v2 offset:0\n"
    "v_mfma_f32_16x16x32_bf16 v[44:47], a[72:75], v[20:23], 0\n"
    "v_cvt_pk_bf16_f32 v84, v60, v61\n"
    "v_cvt_pk_bf16_f32 v85, v62, v63\n"
    "v_mfma_f32_16x16x32_bf16 v[48:51], a[72:75], v[24:27], 0\n"
    "v_cvt_pk_bf16_f32 v86, v68, v69\n"
    "v_cvt_pk_bf16_f32 v87, v70, v71\n"
    "v_mfma_f32_16x16x32_bf16 v[52:55], v[20:23], a[144:147], 0\n"
    "buffer_store_dwordx4 v[84:87], v4, s[44:47], s81 offen sc1\n"
    "v_cvt_pk_bf16_f32 v88, v64, v65\n"
    "v_mfma_f32_16x16x32_bf16 v[56:59], v[24:27], a[144:147], 0\n"
    "s_waitcnt lgkmcnt(0)\n"
    "v_cvt_pk_bf16_f32 v89, v66, v67\n"
    "v_cvt_pk_bf16_f32 v90, v72, v73\n"
    "v_mfma_f32_16x16x32_bf16 v[36:39], a[4:7], v[28:31], v[36:39]\n"
    "s_add_i32 s73, s81, s62\n"
    "ds_read_b128 v[24:27], v1 offset:19456\n"
    "v_cvt_pk_bf16_f32 v91, v74, v75\n"
    "v_mfma_f32_16x16x32_bf16 v[40:43], a[4:7], v[32:35], v[40:43]\n"
    "ds_read_b128 v[20:23], v1 offset:1024\n"
    "v_cvt_pk_bf16_f32 v92, v76, v77\n"
    "v_mfma_f32_16x16x32_bf16 v[44:47], a[76:79], v[28:31], v[44:47]\n"
    "v_cvt_pk_bf16_f32 v93, v78, v79\n"
    "v_cvt_pk_bf16_f32 v94, v80, v81\n"
    "v_mfma_f32_16x16x32_bf16 v[48:51], a[76:79], v[32:35], v[48:51]\n"
    "buffer_store_dwordx4 v[88:91], v4, s[44:47], s73 offen sc1\n"
    "v_cvt_pk_bf16_f32 v95, v82, v83\n"
    "v_mfma_f32_16x16x32_bf16 v[52:55], v[28:31], a[148:151], v[52:55]\n"
    "s_add_i32 s73, s81, s63\n"
    "v_mfma_f32_16x16x32_bf16 v[56:59], v[32:35], a[148:151], v[56:59]\n"
    "s_waitcnt lgkmcnt(0)\n"
    "v_mfma_f32_16x16x32_bf16 v[36:39], a[8:11], v[20:23], v[36:39]\n"
    "ds_read_b128 v[32:35], v2 offset:19456\n"
    "v_mfma_f32_16x16x32_bf16 v[40:43], a[8:11], v[24:27], v[40:43]\n"
    "ds_read_b128 v[28:31], v2 offset:1024\n"
    "v_mfma_f32_16x16x32_bf16 v[44:47], a[80:83], v[20:23], v[44:47]\n"
    "buffer_store_dwordx4 v[92:95], v4, s[44:47], s73 offen sc1\n"
    "v_mfma_f32_16x16x32_bf16 v[48:51], a[80:83], v[24:27], v[48:51]\n"
    "v_mfma_f32_16x16x32_bf16 v[52:55], v[20:23], a[152:155], v[52:55]\n"
    "v_mfma_f32_16x16x32_bf16 v[56:59], v[24:27], a[152:155], v[56:59]\n"
    "s_waitcnt lgkmcnt(0)\n"
    "v_mfma_f32_16x16x32_bf16 v[36:39], a[12:15], v[28:31], v[36:39]\n"
    "ds_read_b128 v[24:27], v1 offset:20480\n"
    "v_mfma_f32_16x16x32_bf16 v[40:43], a[12:15], v[32:35], v[40:43]\n"
    "ds_read_b128 v[20:23], v1 offset:2048\n"
    "v_mfma_f32_16x16x32_bf16 v[44:47], a[84:87], v[28:31], v[44:47]\n"
    "s_waitcnt vmcnt(3)\n"
    "v_add_u32 v16, v16, v11\n"
    "v_mfma_f32_16x16x32_bf16 v[48:51], a[84:87], v[32:35], v[48:51]\n"
    "v_cmp_gt_i32 s[92:93], s68, v16\n"
    "v_mfma_f32_16x16x32_bf16 v[52:55], v[28:31], a[156:159], v[52:55]\n"
    "v_cmp_gt_i32 vcc, s69, v16\n"
    "v_mfma_f32_16x16x32_bf16 v[56:59], v[32:35], a[156:159], v[56:59]\n"
    "s_waitcnt lgkmcnt(0)\n"
    "v_mfma_f32_16x16x32_bf16 v[36:39], a[16:19], v[20:23], v[36:39]\n"
    "s_cmp_eq_u32 vcc_hi, 0\n"
    "s_cselect_b32 s93, 1, 2\n"
    "ds_read_b128 v[32:35], v2 offset:20480\n"
    "v_mfma_f32_16x16x32_bf16 v[40:43], a[16:19], v[24:27], v[40:43]\n"
    "s_cselect_b32 s87, s70, 0\n"
    "s_cmp_eq_u32 s92, 0\n"
    "ds_read_b128 v[28:31], v2 offset:2048\n"
    "v_mfma_f32_16x16x32_bf16 v[44:47], a[88:91], v[20:23], v[44:47]\n"
    "s_cselect_b32 s87, s87, s93\n"
    "v_mov_b32 v17, s87\n"
    "v_mfma_f32_16x16x32_bf16 v[48:51], a[88:91], v[24:27], v[48:51]\n"
    "ds_write_b32 v12, v17\n"
    "v_mfma_f32_16x16x32_bf16 v[52:55], v[20:23], a[160:163], v[52:55]\n"
    "v_mfma_f32_16x16x32_bf16 v[56:59], v[24:27], a[160:163], v[56:59]\n"
    "s_waitcnt lgkmcnt(1)\n"
    "v_mfma_f32_16x16x32_bf16 v[36:39], a[20:23], v[28:31], v[36:39]\n"
    "s_waitcnt lgkmcnt(0)\n"
    "s_barrier\n"
    "ds_read_b32 v16, v10\n"
    "v_mfma_f32_16x16x32_bf16 v[40:43], a[20:23], v[32:35], v[40:43]\n"
    "ds_read_b128 v[24:27], v1 offset:21504\n"
    "ds_read_b128 v[20:23], v1 offset:3072\n"
    "v_mfma_f32_16x16x32_bf16 v[44:47], a[92:95], v[28:31], v[44:47]\n"
    "v_mfma_f32_16x16x32_bf16 v[48:51], a[92:95], v[32:35], v[48:51]\n"
    "v_mfma_f32_16x16x32_bf16 v[52:55], v[28:31], a[164:167], v[52:55]\n"
    "s_waitcnt lgkmcnt(2)\n"
    "v_readfirstlane_b32 s71, v16\n"
    "v_mfma_f32_16x16x32_bf16 v[56:59], v[32:35], a[164:167], v[56:59]\n"
    "s_waitcnt lgkmcnt(0)\n"
    "s_cmp_lg_u32 s71, 2\n"
    "s_cselect_b32 s42, s38, 0\n"
    "v_mfma_f32_16x16x32_bf16 v[36:39], a[24:27], v[20:23], v[36:39]\n"
    "s_cmp_eq_u32 s71, 1\n"
    "s_cselect_b32 s80, s69, s68\n"
    "ds_read_b128 v[32:35], v2 offset:21504\n"
    "v_mfma_f32_16x16x32_bf16 v[40:43], a[24:27], v[24:27], v[40:43]\n"
    "s_cselect_b32 s86, s55, s54\n"
    "s_cselect_b32 s78, s57, s56\n"
    "ds_read_b128 v[28:31], v2 offset:3072\n"
    "v_mfma_f32_16x16x32_bf16 v[44:47], a[96:99], v[20:23], v[44:47]\n"
    "s_mul_i32 s87, s80, s58\n"
    "s_add_i32 s86, s86, s87\n"
    "v_mfma_f32_16x16x32_bf16 v[48:51], a[96:99], v[24:27], v[48:51]\n"
    "v_mfma_f32_16x16x32_bf16 v[52:55], v[20:23], a[168:171], v[52:55]\n"
    "v_mfma_f32_16x16x32_bf16 v[56:59], v[24:27], a[168:171], v[56:59]\n"
    "s_waitcnt lgkmcnt(0)\n"
    "s_add_i32 m0, s66, 36864\n"
    "v_mfma_f32_16x16x32_bf16 v[36:39], a[28:31], v[28:31], v[36:39]\n"
    "ds_read_b128 v[24:27], v1 offset:22528\n"
    "buffer_load_dwordx4 v0, s[40:43], s86 offen offset:0 sc1 lds\n"
    "v_mfma_f32_16x16x32_bf16 v[40:43], a[28:31], v[32:35], v[40:43]\n"
    "ds_read_b128 v[20:23], v1 offset:4096\n"
    "v_mfma_f32_16x16x32_bf16 v[44:47], a[100:103], v[28:31], v[44:47]\n"
    "s_and_b32 s87, s80, 15\n"
    "s_mul_i32 s87, s87, s59\n"
    "s_add_i32 s78, s78, s87\n"
    "v_mfma_f32_16x16x32_bf16 v[48:51], a[100:103], v[32:35], v[48:51]\n"
    "s_add_i32 m0, s66, 37760\n"
    "s_and_b32 s92, s71, 1\n"
    "s_add_i32 s69, s69, s92\n"
    "v_mfma_f32_16x16x32_bf16 v[52:55], v[28:31], a[172:175], v[52:55]\n"
    "s_lshr_b32 s92, 1, s71\n"
    "s_add_i32 s68, s68, s92\n"
    "buffer_load_dwordx4 v0, s[40:43], s86 offen offset:128 sc1 lds\n"
    "v_mfma_f32_16x16x32_bf16 v[56:59], v[32:35], a[172:175], v[56:59]\n"
    "s_waitcnt lgkmcnt(0)\n"
    "s_lshr_b32 s70, 1, s71\n"
    "v_mfma_f32_16x16x32_bf16 v[36:39], a[32:35], v[20:23], v[36:39]\n"
    "ds_read_b128 v[32:35], v2 offset:22528\n"
    "v_mfma_f32_16x16x32_bf16 v[40:43], a[32:35], v[24:27], v[40:43]\n"
    "s_add_i32 m0, s66, 38656\n"
    "ds_read_b128 v[28:31], v2 offset:4096\n"
    "v_mfma_f32_16x16x32_bf16 v[44:47], a[104:107], v[20:23], v[44:47]\n"
    "buffer_load_dwordx4 v0, s[40:43], s86 offen offset:256 sc1 lds\n"
    "v_mfma_f32_16x16x32_bf16 v[48:51], a[104:107], v[24:27], v[48:51]\n"
    "v_mfma_f32_16x16x32_bf16 v[52:55], v[20:23], a[176:179], v[52:55]\n"
    "v_mfma_f32_16x16x32_bf16 v[56:59], v[24:27], a[176:179], v[56:59]\n"
    "s_waitcnt lgkmcnt(0)\n"
    "s_add_i32 m0, s66, 39552\n"
    "v_mfma_f32_16x16x32_bf16 v[36:39], a[36:39], v[28:31], v[36:39]\n"
    "ds_read_b128 v[24:27], v1 offset:23552\n"
    "buffer_load_dwordx4 v0, s[40:43], s86 offen offset:384 sc1 lds\n"
    "v_mfma_f32_16x16x32_bf16 v[40:43], a[36:39], v[32:35], v[40:43]\n"
    "ds_read_b128 v[20:23], v1 offset:5120\n"
    "v_mfma_f32_16x16x32_bf16 v[44:47], a[108:111], v[28:31], v[44:47]\n"
    "v_mfma_f32_16x16x32_bf16 v[48:51], a[108:111], v[32:35], v[48:51]\n"
    "v_mfma_f32_16x16x32_bf16 v[52:55], v[28:31], a[180:183], v[52:55]\n"
    "s_waitcnt vmcnt(4)\n"
    "s_barrier\n"
    "v_mfma_f32_16x16x32_bf16 v[56:59], v[32:35], a[180:183], v[56:59]\n"
    "s_mov_b64 exec, s[90:91]\n"
    "buffer_store_dword v19, off, s[44:47], s82 sc1\n"
    "s_mov_b64 exec, -1\n"
    "s_waitcnt lgkmcnt(0)\n"
    "s_add_i32 m0, s66, 40448\n"
    "v_mfma_f32_16x16x32_bf16 v[36:39], a[40:43], v[20:23], v[36:39]\n"
    "s_mov_b32 s46, s38\n"
    "ds_read_b128 v[32:35], v2 offset:23552\n"
    "buffer_load_dwordx4 v0, s[40:43], s86 offen offset:512 sc1 lds\n"
    "v_mfma_f32_16x16x32_bf16 v[40:43], a[40:43], v[24:27], v[40:43]\n"
    "s_mov_b32 s81, s75\n"
    "s_mov_b32 s82, s76\n"
    "ds_read_b128 v[28:31], v2 offset:5120\n"
    "v_mfma_f32_16x16x32_bf16 v[44:47], a[112:115], v[20:23], v[44:47]\n"
    "s_mov_b32 s83, s77\n"
    "s_mov_b32 s75, s78\n"
    "s_mov_b32 s77, s80\n"
    "v_mfma_f32_16x16x32_bf16 v[48:51], a[112:115], v[24:27], v[48:51]\n"
    "s_mul_i32 s76, s71, s94\n"
    "s_add_i32 s76, s76, s60\n"
    "v_mfma_f32_16x16x32_bf16 v[52:55], v[20:23], a[184:187], v[52:55]\n"
    "v_mfma_f32_16x16x32_bf16 v[56:59], v[24:27], a[184:187], v[56:59]\n"
    "s_waitcnt lgkmcnt(0)\n"
    "s_add_i32 m0, s66, 41344\n"
    "v_mfma_f32_16x16x32_bf16 v[36:39], a[44:47], v[28:31], v[36:39]\n"
    "ds_read_b128 v[24:27], v1 offset:24576\n"
    "buffer_load_dwordx4 v0, s[40:43], s86 offen offset:640 sc1 lds\n"
    "v_mfma_f32_16x16x32_bf16 v[40:43], a[44:47], v[32:35], v[40:43]\n"
    "ds_read_b128 v[20:23], v1 offset:6144\n"
    "v_mfma_f32_16x16x32_bf16 v[44:47], a[116:119], v[28:31], v[44:47]\n"
    "v_mfma_f32_16x16x32_bf16 v[48:51], a[116:119], v[32:35], v[48:51]\n"
    "v_mfma_f32_16x16x32_bf16 v[52:55], v[28:31], a[188:191], v[52:55]\n"
    "v_mfma_f32_16x16x32_bf16 v[56:59], v[32:35], a[188:191], v[56:59]\n"
    "s_waitcnt lgkmcnt(0)\n"
    "s_add_i32 m0, s66, 42240\n"
    "v_mfma_f32_16x16x32_bf16 v[36:39], a[48:51], v[20:23], v[36:39]\n"
    "ds_read_b128 v[32:35], v2 offset:24576\n"
    "buffer_load_dwordx4 v0, s[40:43], s86 offen offset:768 sc1 lds\n"
    "v_mfma_f32_16x16x32_bf16 v[40:43], a[48:51], v[24:27], v[40:43]\n"
    "ds_read_b128 v[28:31], v2 offset:6144\n"
    "v_mfma_f32_16x16x32_bf16 v[44:47], a[120:123], v[20:23], v[44:47]\n"
    "v_mfma_f32_16x16x32_bf16 v[48:51], a[120:123], v[24:27], v[48:51]\n"
    "v_mfma_f32_16x16x32_bf16 v[52:55], v[20:23], a[192:195], v[52:55]\n"
    "v_mfma_f32_16x16x32_bf16 v[56:59], v[24:27], a[192:195], v[56:59]\n"
    "s_waitcnt lgkmcnt(0)\n"
    "s_add_i32 m0, s66, 43136\n"
    "v_mfma_f32_16x16x32_bf16 v[36:39], a[52:55], v[28:31], v[36:39]\n"
    "ds_read_b128 v[24:27], v1 offset:25600\n"
    "buffer_load_dwordx4 v0, s[40:43], s86 offen offset:896 sc1 lds\n"
    "v_mfma_f32_16x16x32_bf16 v[40:43], a[52:55], v[32:35], v[40:43]\n"
    "ds_read_b128 v[20:23], v1 offset:7168\n"
    "v_mfma_f32_16x16x32_bf16 v[44:47], a[124:127], v[28:31], v[44:47]\n"
    "v_mfma_f32_16x16x32_bf16 v[48:51], a[124:127], v[32:35], v[48:51]\n"
    "v_mfma_f32_16x16x32_bf16 v[52:55], v[28:31], a[196:199], v[52:55]\n"
    "v_mfma_f32_16x16x32_bf16 v[56:59], v[32:35], a[196:199], v[56:59]\n"
    "s_waitcnt lgkmcnt(0)\n"
    "s_add_i32 m0, s66, 44032\n"
    "v_mfma_f32_16x16x32_bf16 v[36:39], a[56:59], v[20:23], v[36:39]\n"
    "ds_read_b128 v[32:35], v2 offset:25600\n"
    "buffer_load_dwordx4 v0, s[40:43], s86 offen offset:1024 sc1 lds\n"
    "v_mfma_f32_16x16x32_bf16 v[40:43], a[56:59], v[24:27], v[40:43]\n"
    "ds_read_b128 v[28:31], v2 offset:7168\n"
    "v_mfma_f32_16x16x32_bf16 v[44:47], a[128:131], v[20:23], v[44:47]\n"
    "v_mfma_f32_16x16x32_bf16 v[48:51], a[128:131], v[24:27], v[48:51]\n"
    "v_mfma_f32_16x16x32_bf16 v[52:55], v[20:23], a[200:203], v[52:55]\n"
    "v_mfma_f32_16x16x32_bf16 v[56:59], v[24:27], a[200:203], v[56:59]\n"
    "s_waitcnt lgkmcnt(0)\n"
    "v_mfma_f32_16x16x32_bf16 v[36:39], a[60:63], v[28:31], v[36:39]\n"
    "ds_read_b128 v[24:27], v1 offset:26624\n"
    "v_mfma_f32_16x16x32_bf16 v[40:43], a[60:63], v[32:35], v[40:43]\n"
    "ds_read_b128 v[20:23], v1 offset:8192\n"
    "v_mfma_f32_16x16x32_bf16 v[44:47], a[132:135], v[28:31], v[44:47]\n"
    "v_mfma_f32_16x16x32_bf16 v[48:51], a[132:135], v[32:35], v[48:51]\n"
    "v_mfma_f32_16x16x32_bf16 v[52:55], v[28:31], a[204:207], v[52:55]\n"
    "v_mfma_f32_16x16x32_bf16 v[56:59], v[32:35], a[204:207], v[56:59]\n"
    "s_waitcnt lgkmcnt(0)\n"
    "v_mfma_f32_16x16x32_bf16 v[36:39], a[64:67], v[20:23], v[36:39]\n"
    "ds_read_b128 v[32:35], v2 offset:26624\n"
    "global_load_dword v16, v3, s[52:53] sc1\n"
    "v_mfma_f32_16x16x32_bf16 v[40:43], a[64:67], v[24:27], v[40:43]\n"
    "ds_read_b128 v[28:31], v2 offset:8192\n"
    "v_mfma_f32_16x16x32_bf16 v[44:47], a[136:139], v[20:23], v[44:47]\n"
    "v_mfma_f32_16x16x32_bf16 v[48:51], a[136:139], v[24:27], v[48:51]\n"
    "v_mfma_f32_16x16x32_bf16 v[52:55], v[20:23], a[208:211], v[52:55]\n"
    "v_mfma_f32_16x16x32_bf16 v[56:59], v[24:27], a[208:211], v[56:59]\n"
    "s_waitcnt lgkmcnt(0)\n"
    "v_mfma_f32_16x16x32_bf16 v[36:39], a[68:71], v[28:31], v[36:39]\n"
    "v_mfma_f32_16x16x32_bf16 v[40:43], a[68:71], v[32:35], v[40:43]\n"
    "s_waitcnt vmcnt(6) lgkmcnt(0)\n"
    "s_barrier\n"
    "ds_read_b128 v[24:27], v8 offset:18432\n"
    "v_mfma_f32_16x16x32_bf16 v[44:47], a[140:143], v[28:31], v[44:47]\n"
    "ds_read_b128 v[20:23], v8 offset:0\n"
    "v_mfma_f32_16x16x32_bf16 v[48:51], a[140:143], v[32:35], v[48:51]\n"
    "v_mfma_f32_16x16x32_bf16 v[52:55], v[28:31], a[212:215], v[52:55]\n"
    "v_mfma_f32_16x16x32_bf16 v[56:59], v[32:35], a[212:215], v[56:59]\n"
    "s_cmp_eq_u32 s42, 0\n"
    "s_cbranch_scc1 L_idle1%=\n"
    "L_body1%=:\n"
    "s_waitcnt lgkmcnt(0)\n"
    "v_mfma_f32_16x16x32_bf16 v[60:63], a[0:3], v[20:23], 0\n"
    "ds_read_b128 v[32:35], v9 offset:18432\n"
    "v_mov_b32 v19, s83\n"
    "v_mfma_f32_16x16x32_bf16 v[64:67], a[0:3], v[24:27], 0\n"
    "ds_read_b128 v[28:31], v9 offset:0\n"
    "v_mfma_f32_16x16x32_bf16 v[68:71], a[72:75], v[20:23], 0\n"
    "v_cvt_pk_bf16_f32 v84, v36, v37\n"
    "v_cvt_pk_bf16_f32 v85, v38, v39\n"
    "v_mfma_f32_16x16x32_bf16 v[72:75], a[72:75], v[24:27], 0\n"
    "v_cvt_pk_bf16_f32 v86, v44, v45\n"
    "v_cvt_pk_bf16_f32 v87, v46, v47\n"
    "v_mfma_f32_16x16x32_bf16 v[76:79], v[20:23], a[144:147], 0\n"
    "buffer_store_dwordx4 v[84:87], v4, s[44:47], s81 offen sc1\n"
    "v_cvt_pk_bf16_f32 v88, v40, v41\n"
    "v_mfma_f32_16x16x32_bf16 v[80:83], v[24:27], a[144:147], 0\n"
    "s_waitcnt lgkmcnt(0)\n"
    "v_cvt_pk_bf16_f32 v89, v42, v43\n"
    "v_cvt_pk_bf16_f32 v90, v48, v49\n"
    "v_mfma_f32_16x16x32_bf16 v[60:63], a[4:7], v[28:31], v[60:63]\n"
    "s_add_i32 s73, s81, s62\n"
    "ds_read_b128 v[24:27], v8 offset:19456\n"
    "v_cvt_pk_bf16_f32 v91, v50, v51\n"
    "v_mfma_f32_16x16x32_bf16 v[64:67], a[4:7], v[32:35], v[64:67]\n"
    "ds_read_b128 v[20:23], v8 offset:1024\n"
    "v_cvt_pk_bf16_f32 v92, v52, v53\n"
    "v_mfma_f32_16x16x32_bf16 v[68:71], a[76:79], v[28:31], v[68:71]\n"
    "v_cvt_pk_bf16_f32 v93, v54, v55\n"
    "v_cvt_pk_bf16_f32 v94, v56, v57\n"
    "v_mfma_f32_16x16x32_bf16 v[72:75], a[76:79], v[32:35], v[72:75]\n"
    "buffer_store_dwordx4 v[88:91], v4, s[44:47], s73 offen sc1\n"
    "v_cvt_pk_bf16_f32 v95, v58, v59\n"
    "v_mfma_f32_16x16x32_bf16 v[76:79], v[28:31], a[148:151], v[76:79]\n"
    "s_add_i32 s73, s81, s63\n"
    "v_mfma_f32_16x16x32_bf16 v[80:83], v[32:35], a[148:151], v[80:83]\n"
    "s_waitcnt lgkmcnt(0)\n"
    "v_mfma_f32_16x16x32_bf16 v[60:63], a[8:11], v[20:23], v[60:63]\n"
    "ds_read_b128 v[32:35], v9 offset:19456\n"
    "v_mfma_f32_16x16x32_bf16 v[64:67], a[8:11], v[24:27], v[64:67]\n"
    "ds_read_b128 v[28:31], v9 offset:1024\n"
    "v_mfma_f32_16x16x32_bf16 v[68:71], a[80:83], v[20:23], v[68:71]\n"
    "buffer_store_dwordx4 v[92:95], v4, s[44:47], s73 offen sc1\n"
    "v_mfma_f32_16x16x32_bf16 v[72:75], a[80:83], v[24:27], v[72:75]\n"
    "v_mfma_f32_16x16x32_bf16 v[76:79], v[20:23], a[152:155], v[76:79]\n"
    "v_mfma_f32_16x16x32_bf16 v[80:83], v[24:27], a[152:155], v[80:83]\n"
    "s_waitcnt lgkmcnt(0)\n"
    "v_mfma_f32_16x16x32_bf16 v[60:63], a[12:15], v[28:31], v[60:63]\n"
    "ds_read_b128 v[24:27], v8 offset:20480\n"
    "v_mfma_f32_16x16x32_bf16 v[64:67], a[12:15], v[32:35], v[64:67]\n"
    "ds_read_b128 v[20:23], v8 offset:2048\n"
    "v_mfma_f32_16x16x32_bf16 v[68:71], a[84:87], v[28:31], v[68:71]\n"
    "s_waitcnt vmcnt(3)\n"
    "v_add_u32 v16, v16, v11\n"
    "v_mfma_f32_16x16x32_bf16 v[72:75], a[84:87], v[32:35], v[72:75]\n"
    "v_cmp_gt_i32 s[92:93], s68, v16\n"
    "v_mfma_f32_16x16x32_bf16 v[76:79], v[28:31], a[156:159], v[76:79]\n"
    "v_cmp_gt_i32 vcc, s69, v16\n"
    "v_mfma_f32_16x16x32_bf16 v[80:83], v[32:35], a[156:159], v[80:83]\n"
    "s_waitcnt lgkmcnt(0)\n"
    "v_mfma_f32_16x16x32_bf16 v[60:63], a[16:19], v[20:23], v[60:63]\n"
    "s_cmp_eq_u32 vcc_hi, 0\n"
    "s_cselect_b32 s93, 1, 2\n"
    "ds_read_b128 v[32:35], v9 offset:20480\n"
    "v_mfma_f32_16x16x32_bf16 v[64:67], a[16:19], v[24:27], v[64:67]\n"
    "s_cselect_b32 s87, s70, 0\n"
    "s_cmp_eq_u32 s92, 0\n"
    "ds_read_b128 v[28:31], v9 offset:2048\n"
    "v_mfma_f32_16x16x32_bf16 v[68:71], a[88:91], v[20:23], v[68:71]\n"
    "s_cselect_b32 s87, s87, s93\n"
    "v_mov_b32 v17, s87\n"
    "v_mfma_f32_16x16x32_bf16 v[72:75], a[88:91], v[24:27], v[72:75]\n"
    "ds_write_b32 v12, v17\n"
    "v_mfma_f32_16x16x32_bf16 v[76:79], v[20:23], a[160:163], v[76:79]\n"
    "v_mfma_f32_16x16x32_bf16 v[80:83], v[24:27], a[160:163], v[80:83]\n"
    "s_waitcnt lgkmcnt(1)\n"
    "v_mfma_f32_16x16x32_bf16 v[60:63], a[20:23], v[28:31], v[60:63]\n"
    "s_waitcnt lgkmcnt(0)\n"
    "s_barrier\n"
    "ds_read_b32 v16, v10\n"
    "v_mfma_f32_16x16x32_bf16 v[64:67], a[20:23], v[32:35], v[64:67]\n"
    "ds_read_b128 v[24:27], v8 offset:21504\n"
    "ds_read_b128 v[20:23], v8 offset:3072\n"
    "v_mfma_f32_16x16x32_bf16 v[68:71], a[92:95], v[28:31], v[68:71]\n"
    "v_mfma_f32_16x16x32_bf16 v[72:75], a[92:95], v[32:35], v[72:75]\n"
    "v_mfma_f32_16x16x32_bf16 v[76:79], v[28:31], a[164:167], v[76:79]\n"
    "s_waitcnt lgkmcnt(2)\n"
    "v_readfirstlane_b32 s71, v16\n"
    "v_mfma_f32_16x16x32_bf16 v[80:83], v[32:35], a[164:167], v[80:83]\n"
    "s_waitcnt lgkmcnt(0)\n"
    "s_cmp_lg_u32 s71, 2\n"
    "s_cselect_b32 s42, s38, 0\n"
    "v_mfma_f32_16x16x32_bf16 v[60:63], a[24:27], v[20:23], v[60:63]\n"
    "s_cmp_eq_u32 s71, 1\n"
    "s_cselect_b32 s80, s69, s68\n"
    "ds_read_b128 v[32:35], v9 offset:21504\n"
    "v_mfma_f32_16x16x32_bf16 v[64:67], a[24:27], v[24:27], v[64:67]\n"
    "s_cselect_b32 s86, s55, s54\n"
    "s_cselect_b32 s78, s57, s56\n"
    "ds_read_b128 v[28:31], v9 offset:3072\n"
    "v_mfma_f32_16x16x32_bf16 v[68:71], a[96:99], v[20:23], v[68:71]\n"
    "s_mul_i32 s87, s80, s58\n"
    "s_add_i32 s86, s86, s87\n"
    "v_mfma_f32_16x16x32_bf16 v[72:75], a[96:99], v[24:27], v[72:75]\n"
    "v_mfma_f32_16x16x32_bf16 v[76:79], v[20:23], a[168:171], v[76:79]\n"
    "v_mfma_f32_16x16x32_bf16 v[80:83], v[24:27], a[168:171], v[80:83]\n"
    "s_waitcnt lgkmcnt(0)\n"
    "s_add_i32 m0, s66, 0\n"
    "v_mfma_f32_16x16x32_bf16 v[60:63], a[28:31], v[28:31], v[60:63]\n"
    "ds_read_b128 v[24:27], v8 offset:22528\n"
    "buffer_load_dwordx4 v0, s[40:43], s86 offen offset:0 sc1 lds\n"
    "v_mfma_f32_16x16x32_bf16 v[64:67], a[28:31], v[32:35], v[64:67]\n"
    "ds_read_b128 v[20:23], v8 offset:4096\n"
    "v_mfma_f32_16x16x32_bf16 v[68:71], a[100:103], v[28:31], v[68:71]\n"
    "s_and_b32 s87, s80, 15\n"
    "s_mul_i32 s87, s87, s59\n"
    "s_add_i32 s78, s78, s87\n"
    "v_mfma_f32_16x16x32_bf16 v[72:75], a[100:103], v[32:35], v[72:75]\n"
    "s_add_i32 m0, s66, 896\n"
    "s_and_b32 s92, s71, 1\n"
    "s_add_i32 s69, s69, s92\n"
    "v_mfma_f32_16x16x32_bf16 v[76:79], v[28:31], a[172:175], v[76:79]\n"
    "s_lshr_b32 s92, 1, s71\n"
    "s_add_i32 s68, s68, s92\n"
    "buffer_load_dwordx4 v0, s[40:43], s86 offen offset:128 sc1 lds\n"
    "v_mfma_f32_16x16x32_bf16 v[80:83], v[32:35], a[172:175], v[80:83]\n"
    "s_waitcnt lgkmcnt(0)\n"
    "s_lshr_b32 s70, 1, s71\n"
    "v_mfma_f32_16x16x32_bf16 v[60:63], a[32:35], v[20:23], v[60:63]\n"
    "ds_read_b128 v[32:35], v9 offset:22528\n"
    "v_mfma_f32_16x16x32_bf16 v[64:67], a[32:35], v[24:27], v[64:67]\n"
    "s_add_i32 m0, s66, 1792\n"
    "ds_read_b128 v[28:31], v9 offset:4096\n"
    "v_mfma_f32_16x16x32_bf16 v[68:71], a[104:107], v[20:23], v[68:71]\n"
    "buffer_load_dwordx4 v0, s[40:43], s86 offen offset:256 sc1 lds\n"
    "v_mfma_f32_16x16x32_bf16 v[72:75], a[104:107], v[24:27], v[72:75]\n"
    "v_mfma_f32_16x16x32_bf16 v[76:79], v[20:23], a[176:179], v[76:79]\n"
    "v_mfma_f32_16x16x32_bf16 v[80:83], v[24:27], a[176:179], v[80:83]\n"
    "s_waitcnt lgkmcnt(0)\n"
    "s_add_i32 m0, s66, 2688\n"
    "v_mfma_f32_16x16x32_bf16 v[60:63], a[36:39], v[28:31], v[60:63]\n"
    "ds_read_b128 v[24:27], v8 offset:23552\n"
    "buffer_load_dwordx4 v0, s[40:43], s86 offen offset:384 sc1 lds\n"
    "v_mfma_f32_16x16x32_bf16 v[64:67], a[36:39], v[32:35], v[64:67]\n"
    "ds_read_b128 v[20:23], v8 offset:5120\n"
    "v_mfma_f32_16x16x32_bf16 v[68:71], a[108:111], v[28:31], v[68:71]\n"
    "v_mfma_f32_16x16x32_bf16 v[72:75], a[108:111], v[32:35], v[72:75]\n"
    "v_mfma_f32_16x16x32_bf16 v[76:79], v[28:31], a[180:183], v[76:79]\n"
    "s_waitcnt vmcnt(4)\n"
    "s_barrier\n"
    "v_mfma_f32_16x16x32_bf16 v[80:83], v[32:35], a[180:183], v[80:83]\n"
    "s_mov_b64 exec, s[90:91]\n"
    "buffer_store_dword v19, off, s[44:47], s82 sc1\n"
    "s_mov_b64 exec, -1\n"
    "s_waitcnt lgkmcnt(0)\n"
    "s_add_i32 m0, s66, 3584\n"
    "v_mfma_f32_16x16x32_bf16 v[60:63], a[40:43], v[20:23], v[60:63]\n"
    "s_mov_b32 s46, s38\n"
    "ds_read_b128 v[32:35], v9 offset:23552\n"
    "buffer_load_dwordx4 v0, s[40:43], s86 offen offset:512 sc1 lds\n"
    "v_mfma_f32_16x16x32_bf16 v[64:67], a[40:43], v[24:27], v[64:67]\n"
    "s_mov_b32 s81, s75\n"
    "s_mov_b32 s82, s76\n"
    "ds_read_b128 v[28:31], v9 offset:5120\n"
    "v_mfma_f32_16x16x32_bf16 v[68:71], a[112:115], v[20:23], v[68:71]\n"
    "s_mov_b32 s83, s77\n"
    "s_mov_b32 s75, s78\n"
    "s_mov_b32 s77, s80\n"
    "v_mfma_f32_16x16x32_bf16 v[72:75], a[112:115], v[24:27], v[72:75]\n"
    "s_mul_i32 s76, s71, s94\n"
    "s_add_i32 s76, s76, s60\n"
    "s_add_i32 s72, s72, 2\n"
    "v_mfma_f32_16x16x32_bf16 v[76:79], v[20:23], a[184:187], v[76:79]\n"
    "v_mfma_f32_16x16x32_bf16 v[80:83], v[24:27], a[184:187], v[80:83]\n"
    "s_waitcnt lgkmcnt(0)\n"
    "s_add_i32 m0, s66, 4480\n"
    "v_mfma_f32_16x16x32_bf16 v[60:63], a[44:47], v[28:31], v[60:63]\n"
    "ds_read_b128 v[24:27], v8 offset:24576\n"
    "buffer_load_dwordx4 v0, s[40:43], s86 offen offset:640 sc1 lds\n"
    "v_mfma_f32_16x16x32_bf16 v[64:67], a[44:47], v[32:35], v[64:67]\n"
    "ds_read_b128 v[20:23], v8 offset:6144\n"
    "v_mfma_f32_16x16x32_bf16 v[68:71], a[116:119], v[28:31], v[68:71]\n"
    "v_mfma_f32_16x16x32_bf16 v[72:75], a[116:119], v[32:35], v[72:75]\n"
    "v_mfma_f32_16x16x32_bf16 v[76:79], v[28:31], a[188:191], v[76:79]\n"
    "v_mfma_f32_16x16x32_bf16 v[80:83], v[32:35], a[188:191], v[80:83]\n"
    "s_waitcnt lgkmcnt(0)\n"
    "s_add_i32 m0, s66, 5376\n"
    "v_mfma_f32_16x16x32_bf16 v[60:63], a[48:51], v[20:23], v[60:63]\n"
    "ds_read_b128 v[32:35], v9 offset:24576\n"
    "buffer_load_dwordx4 v0, s[40:43], s86 offen offset:768 sc1 lds\n"
    "v_mfma_f32_16x16x32_bf16 v[64:67], a[48:51], v[24:27], v[64:67]\n"
    "ds_read_b128 v[28:31], v9 offset:6144\n"
    "v_mfma_f32_16x16x32_bf16 v[68:71], a[120:123], v[20:23], v[68:71]\n"
    "v_mfma_f32_16x16x32_bf16 v[72:75], a[120:123], v[24:27], v[72:75]\n"
    "v_mfma_f32_16x16x32_bf16 v[76:79], v[20:23], a[192:195], v[76:79]\n"
    "v_mfma_f32_16x16x32_bf16 v[80:83], v[24:27], a[192:195], v[80:83]\n"
    "s_waitcnt lgkmcnt(0)\n"
    "s_add_i32 m0, s66, 6272\n"
    "v_mfma_f32_16x16x32_bf16 v[60:63], a[52:55], v[28:31], v[60:63]\n"
    "ds_read_b128 v[24:27], v8 offset:25600\n"
    "buffer_load_dwordx4 v0, s[40:43], s86 offen offset:896 sc1 lds\n"
    "v_mfma_f32_16x16x32_bf16 v[64:67], a[52:55], v[32:35], v[64:67]\n"
    "ds_read_b128 v[20:23], v8 offset:7168\n"
    "v_mfma_f32_16x16x32_bf16 v[68:71], a[124:127], v[28:31], v[68:71]\n"
    "v_mfma_f32_16x16x32_bf16 v[72:75], a[124:127], v[32:35], v[72:75]\n"
    "v_mfma_f32_16x16x32_bf16 v[76:79], v[28:31], a[196:199], v[76:79]\n"
    "v_mfma_f32_16x16x32_bf16 v[80:83], v[32:35], a[196:199], v[80:83]\n"
    "s_waitcnt lgkmcnt(0)\n"
    "s_add_i32 m0, s66, 7168\n"
    "v_mfma_f32_16x16x32_bf16 v[60:63], a[56:59], v[20:23], v[60:63]\n"
    "ds_read_b128 v[32:35], v9 offset:25600\n"
    "buffer_load_dwordx4 v0, s[40:43], s86 offen offset:1024 sc1 lds\n"
    "v_mfma_f32_16x16x32_bf16 v[64:67], a[56:59], v[24:27], v[64:67]\n"
    "ds_read_b128 v[28:31], v9 offset:7168\n"
    "v_mfma_f32_16x16x32_bf16 v[68:71], a[128:131], v[20:23], v[68:71]\n"
    "v_mfma_f32_16x16x32_bf16 v[72:75], a[128:131], v[24:27], v[72:75]\n"
    "v_mfma_f32_16x16x32_bf16 v[76:79], v[20:23], a[200:203], v[76:79]\n"
    "v_mfma_f32_16x16x32_bf16 v[80:83], v[24:27], a[200:203], v[80:83]\n"
    "s_waitcnt lgkmcnt(0)\n"
    "v_mfma_f32_16x16x32_bf16 v[60:63], a[60:63], v[28:31], v[60:63]\n"
    "ds_read_b128 v[24:27], v8 offset:26624\n"
    "v_mfma_f32_16x16x32_bf16 v[64:67], a[60:63], v[32:35], v[64:67]\n"
    "ds_read_b128 v[20:23], v8 offset:8192\n"
    "v_mfma_f32_16x16x32_bf16 v[68:71], a[132:135], v[28:31], v[68:71]\n"
    "v_mfma_f32_16x16x32_bf16 v[72:75], a[132:135], v[32:35], v[72:75]\n"
    "v_mfma_f32_16x16x32_bf16 v[76:79], v[28:31], a[204:207], v[76:79]\n"
    "v_mfma_f32_16x16x32_bf16 v[80:83], v[32:35], a[204:207], v[80:83]\n"
    "s_waitcnt lgkmcnt(0)\n"
    "v_mfma_f32_16x16x32_bf16 v[60:63], a[64:67], v[20:23], v[60:63]\n"
    "ds_read_b128 v[32:35], v9 offset:26624\n"
    "global_load_dword v16, v3, s[52:53] sc1\n"
    "v_mfma_f32_16x16x32_bf16 v[64:67], a[64:67], v[24:27], v[64:67]\n"
    "ds_read_b128 v[28:31], v9 offset:8192\n"
    "v_mfma_f32_16x16x32_bf16 v[68:71], a[136:139], v[20:23], v[68:71]\n"
    "v_mfma_f32_16x16x32_bf16 v[72:75], a[136:139], v[24:27], v[72:75]\n"
    "v_mfma_f32_16x16x32_bf16 v[76:79], v[20:23], a[208:211], v[76:79]\n"
    "v_mfma_f32_16x16x32_bf16 v[80:83], v[24:27], a[208:211], v[80:83]\n"
    "s_waitcnt lgkmcnt(0)\n"
    "v_mfma_f32_16x16x32_bf16 v[60:63], a[68:71], v[28:31], v[60:63]\n"
    "v_mfma_f32_16x16x32_bf16 v[64:67], a[68:71], v[32:35], v[64:67]\n"
    "s_waitcnt vmcnt(6) lgkmcnt(0)\n"
    "s_barrier\n"
    "ds_read_b128 v[24:27], v1 offset:18432\n"
    "v_mfma_f32_16x16x32_bf16 v[68:71], a[140:143], v[28:31], v[68:71]\n"
    "ds_read_b128 v[20:23], v1 offset:0\n"
    "v_mfma_f32_16x16x32_bf16 v[72:75], a[140:143], v[32:35], v[72:75]\n"
    "v_mfma_f32_16x16x32_bf16 v[76:79], v[28:31], a[212:215], v[76:79]\n"
    "v_mfma_f32_16x16x32_bf16 v[80:83], v[32:35], a[212:215], v[80:83]\n"
    "s_cmp_eq_u32 s42, 0\n"
    "s_cbranch_scc0 L_body0%=\n"
    "L_idle0%=:\n"
    "s_nop 4\n"
    "v_mov_b32 v19, s83\n"
    "v_cvt_pk_bf16_f32 v84, v60, v61\n"
    "v_cvt_pk_bf16_f32 v85, v62, v63\n"
    "v_cvt_pk_bf16_f32 v86, v68, v69\n"
    "v_cvt_pk_bf16_f32 v87, v70, v71\n"
    "buffer_store_dwordx4 v[84:87], v4, s[44:47], s81 offen sc1\n"
    "v_cvt_pk_bf16_f32 v88, v64, v65\n"
    "v_cvt_pk_bf16_f32 v89, v66, v67\n"
    "v_cvt_pk_bf16_f32 v90, v72, v73\n"
    "v_cvt_pk_bf16_f32 v91, v74, v75\n"
    "s_add_i32 s73, s81, s62\n"
    "buffer_store_dwordx4 v[88:91], v4, s[44:47], s73 offen sc1\n"
    "v_cvt_pk_bf16_f32 v92, v76, v77\n"
    "v_cvt_pk_bf16_f32 v93, v78, v79\n"
    "v_cvt_pk_bf16_f32 v94, v80, v81\n"
    "v_cvt_pk_bf16_f32 v95, v82, v83\n"
    "s_add_i32 s73, s81, s63\n"
    "buffer_store_dwordx4 v[92:95], v4, s[44:47], s73 offen sc1\n"
    "s_waitcnt vmcnt(3)\n"
    "v_add_u32 v16, v16, v11\n"
    "v_cmp_gt_i32 s[92:93], s68, v16\n"
    "v_cmp_gt_i32 vcc, s69, v16\n"
    "s_cmp_eq_u32 vcc_hi, 0\n"
    "s_cselect_b32 s93, 1, 2\n"
    "s_cselect_b32 s87, s70, 0\n"
    "s_cmp_eq_u32 s92, 0\n"
    "s_cselect_b32 s87, s87, s93\n"
    "v_mov_b32 v17, s87\n"
    "ds_write_b32 v12, v17 offset:16\n"
    "s_waitcnt lgkmcnt(0)\n"
    "s_barrier\n"
    "ds_read_b32 v16, v10 offset:16\n"
    "s_waitcnt lgkmcnt(0)\n"
    "v_readfirstlane_b32 s71, v16\n"
    "s_cmp_lg_u32 s71, 2\n"
    "s_cbranch_scc1 L_go0%=\n"
    "s_waitcnt vmcnt(0)\n"
    "s_barrier\n"
    "s_mov_b64 exec, s[90:91]\n"
    "buffer_store_dword v19, off, s[44:47], s82 sc1\n"
    "s_mov_b64 exec, -1\n"
    "s_mov_b32 s46, 0\n"
    "s_cmp_ge_u32 s72, 128\n"
    "s_cbranch_scc1 L_exit%=\n"
    "L_start%=:\n"
    "s_cmp_lg_u32 s65, 0\n"
    "s_cbranch_scc1 L_got0%=\n"
    "L_poll0%=:\n"
    "global_load_dword v16, v3, s[52:53] sc1\n"
    "s_waitcnt vmcnt(0)\n"
    "v_add_u32 v16, v16, v11\n"
    "v_cmp_gt_i32 s[92:93], s68, v16\n"
    "v_cmp_gt_i32 vcc, s69, v16\n"
    "s_cmp_eq_u32 vcc_hi, 0\n"
    "s_cselect_b32 s93, 1, 2\n"
    "s_cselect_b32 s87, s70, 0\n"
    "s_cmp_eq_u32 s92, 0\n"
    "s_cselect_b32 s87, s87, s93\n"
    "s_cmp_lg_u32 s87, 2\n"
    "s_cbranch_scc1 L_rdy0%=\n"
    "s_memrealtime s[88:89]\n"
    "s_waitcnt lgkmcnt(0)\n"
    "s_sub_i32 s74, s64, s88\n"
    "s_cmp_ge_u32 s68, 64\n"
    "s_cselect_b32 s87, 1, 0\n"
    "s_cmp_lt_i32 s74, 0\n"
    "s_cbranch_scc1 L_rdy0%=\n"
    "s_sleep 2\n"
    "s_branch L_poll0%=\n"
    "L_rdy0%=:\n"
    "v_mov_b32 v17, s87\n"
    "ds_write_b32 v10, v17 offset:32\n"
    "s_waitcnt lgkmcnt(0)\n"
    "L_got0%=:\n"
    "s_barrier\n"
    "ds_read_b32 v16, v10 offset:32\n"
    "s_waitcnt lgkmcnt(0)\n"
    "v_readfirstlane_b32 s71, v16\n"
    "L_go0%=:\n"
    "s_cmp_eq_u32 s71, 1\n"
    "s_cselect_b32 s80, s69, s68\n"
    "s_cselect_b32 s86, s55, s54\n"
    "s_cselect_b32 s75, s57, s56\n"
    "s_mul_i32 s87, s80, s58\n"
    "s_add_i32 s86, s86, s87\n"
    "s_add_i32 m0, s66, 0\n"
    "s_nop 0\n"
    "buffer_load_dwordx4 v0, s[36:39], s86 offen offset:0 sc1 lds\n"
    "s_add_i32 m0, s66, 896\n"
    "s_nop 0\n"
    "buffer_load_dwordx4 v0, s[36:39], s86 offen offset:128 sc1 lds\n"
    "s_add_i32 m0, s66, 1792\n"
    "s_nop 0\n"
    "buffer_load_dwordx4 v0, s[36:39], s86 offen offset:256 sc1 lds\n"
    "s_add_i32 m0, s66, 2688\n"
    "s_nop 0\n"
    "buffer_load_dwordx4 v0, s[36:39], s86 offen offset:384 sc1 lds\n"
    "s_add_i32 m0, s66, 3584\n"
    "s_nop 0\n"
    "buffer_load_dwordx4 v0, s[36:39], s86 offen offset:512 sc1 lds\n"
    "s_add_i32 m0, s66, 4480\n"
    "s_nop 0\n"
    "buffer_load_dwordx4 v0, s[36:39], s86 offen offset:640 sc1 lds\n"
    "s_add_i32 m0, s66, 5376\n"
    "s_nop 0\n"
    "buffer_load_dwordx4 v0, s[36:39], s86 offen offset:768 sc1 lds\n"
    "s_add_i32 m0, s66, 6272\n"
    "s_nop 0\n"
    "buffer_load_dwordx4 v0, s[36:39], s86 offen offset:896 sc1 lds\n"
    "s_add_i32 m0, s66, 7168\n"
    "s_nop 0\n"
    "buffer_load_dwordx4 v0, s[36:39], s86 offen offset:1024 sc1 lds\n"
    "s_and_b32 s87, s80, 15\n"
    "s_mul_i32 s87, s87, s59\n"
    "s_add_i32 s75, s75, s87\n"
    "s_and_b32 s92, s71, 1\n"
    "s_add_i32 s69, s69, s92\n"
    "s_lshr_b32 s92, 1, s71\n"
    "s_add_i32 s68, s68, s92\n"
    "s_lshr_b32 s70, 1, s71\n"
    "s_mov_b32 s77, s80\n"
    "s_mul_i32 s76, s71, s94\n"
    "s_add_i32 s76, s76, s60\n"
    "s_waitcnt vmcnt(5)\n"
    "s_barrier\n"
    "ds_read_b128 v[24:27], v1 offset:18432\n"
    "ds_read_b128 v[20:23], v1 offset:0\n"
    "global_load_dword v16, v3, s[52:53] sc1\n"
    "s_mov_b64 exec, s[90:91]\n"
    "buffer_store_dword v19, off, s[44:47], s82 sc1\n"
    "s_mov_b64 exec, -1\n"
    "s_mov_b32 s46, 0\n"
    "s_branch L_body0%=\n"
    "L_idle1%=:\n"
    "s_nop 4\n"
    "v_mov_b32 v19, s83\n"
    "v_cvt_pk_bf16_f32 v84, v36, v37\n"
    "v_cvt_pk_bf16_f32 v85, v38, v39\n"
    "v_cvt_pk_bf16_f32 v86, v44, v45\n"
    "v_cvt_pk_bf16_f32 v87, v46, v47\n"
    "buffer_store_dwordx4 v[84:87], v4, s[44:47], s81 offen sc1\n"
    "v_cvt_pk_bf16_f32 v88, v40, v41\n"
    "v_cvt_pk_bf16_f32 v89, v42, v43\n"
    "v_cvt_pk_bf16_f32 v90, v48, v49\n"
    "v_cvt_pk_bf16_f32 v91, v50, v51\n"
    "s_add_i32 s73, s81, s62\n"
    "buffer_store_dwordx4 v[88:91], v4, s[44:47], s73 offen sc1\n"
    "v_cvt_pk_bf16_f32 v92, v52, v53\n"
    "v_cvt_pk_bf16_f32 v93, v54, v55\n"
    "v_cvt_pk_bf16_f32 v94, v56, v57\n"
    "v_cvt_pk_bf16_f32 v95, v58, v59\n"
    "s_add_i32 s73, s81, s63\n"
    "buffer_store_dwordx4 v[92:95], v4, s[44:47], s73 offen sc1\n"
    "s_waitcnt vmcnt(3)\n"
    "v_add_u32 v16, v16, v11\n"
    "v_cmp_gt_i32 s[92:93], s68, v16\n"
    "v_cmp_gt_i32 vcc, s69, v16\n"
    "s_cmp_eq_u32 vcc_hi, 0\n"
    "s_cselect_b32 s93, 1, 2\n"
    "s_cselect_b32 s87, s70, 0\n"
    "s_cmp_eq_u32 s92, 0\n"
    "s_cselect_b32 s87, s87, s93\n"
    "v_mov_b32 v17, s87\n"
    "ds_write_b32 v12, v17 offset:16\n"
    "s_waitcnt lgkmcnt(0)\n"
    "s_barrier\n"
    "ds_read_b32 v16, v10 offset:16\n"
    "s_waitcnt lgkmcnt(0)\n"
    "v_readfirstlane_b32 s71, v16\n"
    "s_cmp_lg_u32 s71, 2\n"
    "s_cbranch_scc1 L_go1%=\n"
    "s_waitcnt vmcnt(0)\n"
    "s_barrier\n"
    "s_mov_b64 exec, s[90:91]\n"
    "buffer_store_dword v19, off, s[44:47], s82 sc1\n"
    "s_mov_b64 exec, -1\n"
    "s_mov_b32 s46, 0\n"
    "s_cmp_lg_u32 s65, 0\n"
    "s_cbranch_scc1 L_got1%=\n"
    "L_poll1%=:\n"
    "global_load_dword v16, v3, s[52:53] sc1\n"
    "s_waitcnt vmcnt(0)\n"
    "v_add_u32 v16, v16, v11\n"
    "v_cmp_gt_i32 s[92:93], s68, v16\n"
    "v_cmp_gt_i32 vcc, s69, v16\n"
    "s_cmp_eq_u32 vcc_hi, 0\n"
    "s_cselect_b32 s93, 1, 2\n"
    "s_cselect_b32 s87, s70, 0\n"
    "s_cmp_eq_u32 s92, 0\n"
    "s_cselect_b32 s87, s87, s93\n"
    "s_cmp_lg_u32 s87, 2\n"
    "s_cbranch_scc1 L_rdy1%=\n"
    "s_memrealtime s[88:89]\n"
    "s_waitcnt lgkmcnt(0)\n"
    "s_sub_i32 s74, s64, s88\n"
    "s_cmp_ge_u32 s68, 64\n"
    "s_cselect_b32 s87, 1, 0\n"
    "s_cmp_lt_i32 s74, 0\n"
    "s_cbranch_scc1 L_rdy1%=\n"
    "s_sleep 2\n"
    "s_branch L_poll1%=\n"
    "L_rdy1%=:\n"
    "v_mov_b32 v17, s87\n"
    "ds_write_b32 v10, v17 offset:32\n"
    "s_waitcnt lgkmcnt(0)\n"
    "L_got1%=:\n"
    "s_barrier\n"
    "ds_read_b32 v16, v10 offset:32\n"
    "s_waitcnt lgkmcnt(0)\n"
    "v_readfirstlane_b32 s71, v16\n"
    "L_go1%=:\n"
    "s_cmp_eq_u32 s71, 1\n"
    "s_cselect_b32 s80, s69, s68\n"
    "s_cselect_b32 s86, s55, s54\n"
    "s_cselect_b32 s75, s57, s56\n"
    "s_mul_i32 s87, s80, s58\n"
    "s_add_i32 s86, s86, s87\n"
    "s_add_i32 m0, s66, 36864\n"
    "s_nop 0\n"
    "buffer_load_dwordx4 v0, s[36:39], s86 offen offset:0 sc1 lds\n"
    "s_add_i32 m0, s66, 37760\n"
    "s_nop 0\n"
    "buffer_load_dwordx4 v0, s[36:39], s86 offen offset:128 sc1 lds\n"
    "s_add_i32 m0, s66, 38656\n"
    "s_nop 0\n"
    "buffer_load_dwordx4 v0, s[36:39], s86 offen offset:256 sc1 lds\n"
    "s_add_i32 m0, s66, 39552\n"
    "s_nop 0\n"
    "buffer_load_dwordx4 v0, s[36:39], s86 offen offset:384 sc1 lds\n"
    "s_add_i32 m0, s66, 40448\n"
    "s_nop 0\n"
    "buffer_load_dwordx4 v0, s[36:39], s86 offen offset:512 sc1 lds\n"
    "s_add_i32 m0, s66, 41344\n"
    "s_nop 0\n"
    "buffer_load_dwordx4 v0, s[36:39], s86 offen offset:640 sc1 lds\n"
    "s_add_i32 m0, s66, 42240\n"
    "s_nop 0\n"
    "buffer_load_dwordx4 v0, s[36:39], s86 offen offset:768 sc1 lds\n"
    "s_add_i32 m0, s66, 43136\n"
    "s_nop 0\n"
    "buffer_load_dwordx4 v0, s[36:39], s86 offen offset:896 sc1 lds\n"
    "s_add_i32 m0, s66, 44032\n"
    "s_nop 0\n"
    "buffer_load_dwordx4 v0, s[36:39], s86 offen offset:1024 sc1 lds\n"
    "s_and_b32 s87, s80, 15\n"
    "s_mul_i32 s87, s87, s59\n"
    "s_add_i32 s75, s75, s87\n"
    "s_and_b32 s92, s71, 1\n"
    "s_add_i32 s69, s69, s92\n"
    "s_lshr_b32 s92, 1, s71\n"
    "s_add_i32 s68, s68, s92\n"
    "s_lshr_b32 s70, 1, s71\n"
    "s_mov_b32 s77, s80\n"
    "s_mul_i32 s76, s71, s94\n"
    "s_add_i32 s76, s76, s60\n"
    "s_waitcnt vmcnt(5)\n"
    "s_barrier\n"
    "ds_read_b128 v[24:27], v8 offset:18432\n"
    "ds_read_b128 v[20:23], v8 offset:0\n"
    "global_load_dword v16, v3, s[52:53] sc1\n"
    "s_mov_b64 exec, s[90:91]\n"
    "buffer_store_dword v19, off, s[44:47], s82 sc1\n"
    "s_mov_b64 exec, -1\n"
    "s_mov_b32 s46, 0\n"
    "s_branch L_body1%=\n"
    "L_exit%=:\n"
    : GEMM_OUT : GEMM_IN, GEMM_IN2 : GEMM_CLOBBERS);
  if constexpr (MODE == M_GU) asm volatile(
    "s_nop 4\n"
    "s_mov_b32 s40, s36\n"
    "s_mov_b32 s41, s37\n"
    "s_mov_b32 s42, 0\n"
    "s_mov_b32 s43, s39\n"
    "s_mov_b32 s44, s36\n"
    "s_mov_b32 s45, s37\n"
    "s_mov_b32 s46, 0\n"
    "s_mov_b32 s47, s39\n"
    "s_mov_b32 s68, 0\n"
    "s_mov_b32 s69, 0\n"
    "s_mov_b32 s70, 0\n"
    "s_mov_b32 s72, 0\n"
    "s_mov_b32 s81, 0\n"
    "s_mov_b32 s82, 0\n"
    "s_mov_b32 s83, 0\n"
    "s_mov_b32 s91, 0\n"
    "s_lshr_b32 s90, 1, s65\n"
    "s_lshl_b32 s73, s65, 2\n"
    "v_add_u32 v12, s73, v10\n"
    "v_mov_b32 v18, 0xbdd2d3e8\n"
    "s_branch L_start%=\n"
    "L_body0%=:\n"
    "s_waitcnt lgkmcnt(2)\n"
    "v_mfma_f32_16x16x32_bf16 v[20:23], a[0:3], v[112:115], 0\n"
    "ds_read_b128 v[128:131], v1 offset:2048\n"
    "v_mov_b32 v19, s83\n"
    "v_mfma_f32_16x16x32_bf16 v[28:31], a[72:75], v[112:115], 0\n"
    "buffer_store_dwordx4 v[48:51], v5, s[44:47], s81 offen sc1\n"
    "v_mfma_f32_16x16x32_bf16 v[20:23], a[4:7], v[116:119], v[20:23]\n"
    "ds_read_b128 v[132:135], v2 offset:2048\n"
    "v_mul_f32 v56, v24, v24\n"
    "v_mfma_f32_16x16x32_bf16 v[28:31], a[76:79], v[116:119], v[28:31]\n"
    "s_waitcnt lgkmcnt(2)\n"
    "v_fmaak_f32 v56, v18, v56, 0xc0135761\n"
    "v_mul_f32 v56, v56, v24\n"
    "v_mfma_f32_16x16x32_bf16 v[20:23], a[8:11], v[120:123], v[20:23]\n"
    "ds_read_b128 v[136:139], v1 offset:3072\n"
    "v_mul_f32 v68, v24, v32\n"
    "v_mfma_f32_16x16x32_bf16 v[28:31], a[80:83], v[120:123], v[28:31]\n"
    "v_exp_f32 v56, v56\n"
    "v_mfma_f32_16x16x32_bf16 v[20:23], a[12:15], v[124:127], v[20:23]\n"
    "ds_read_b128 v[140:143], v2 offset:3072\n"
    "v_add_f32 v56, 1.0, v56\n"
    "v_mfma_f32_16x16x32_bf16 v[28:31], a[84:87], v[124:127], v[28:31]\n"
    "s_waitcnt lgkmcnt(2)\n"
    "v_rcp_f32 v56, v56\n"
    "v_mfma_f32_16x16x32_bf16 v[20:23], a[16:19], v[128:131], v[20:23]\n"
    "ds_read_b128 v[144:147], v1 offset:4096\n"
    "v_mul_f32 v57, v25, v25\n"
    "v_mfma_f32_16x16x32_bf16 v[28:31], a[88:91], v[128:131], v[28:31]\n"
    "v_fmaak_f32 v57, v18, v57, 0xc0135761\n"
    "v_mul_f32 v57, v57, v25\n"
    "v_mfma_f32_16x16x32_bf16 v[20:23], a[20:23], v[132:135], v[20:23]\n"
    "ds_read_b128 v[148:151], v2 offset:4096\n"
    "v_mul_f32 v69, v25, v33\n"
    "v_mfma_f32_16x16x32_bf16 v[28:31], a[92:95], v[132:135], v[28:31]\n"
    "s_waitcnt lgkmcnt(2)\n"
    "v_exp_f32 v57, v57\n"
    "v_mfma_f32_16x16x32_bf16 v[20:23], a[24:27], v[136:139], v[20:23]\n"
    "ds_read_b128 v[152:155], v1 offset:5120\n"
    "v_add_f32 v57, 1.0, v57\n"
    "v_mfma_f32_16x16x32_bf16 v[28:31], a[96:99], v[136:139], v[28:31]\n"
    "v_rcp_f32 v57, v57\n"
    "v_mfma_f32_16x16x32_bf16 v[20:23], a[28:31], v[140:143], v[20:23]\n"
    "ds_read_b128 v[156:159], v2 offset:5120\n"
    "v_mul_f32 v58, v26, v26\n"
    "v_mfma_f32_16x16x32_bf16 v[28:31], a[100:103], v[140:143], v[28:31]\n"
    "s_waitcnt lgkmcnt(2)\n"
    "v_fmaak_f32 v58, v18, v58, 0xc0135761\n"
    "v_mul_f32 v58, v58, v26\n"
    "v_mfma_f32_16x16x32_bf16 v[20:23], a[32:35], v[144:147], v[20:23]\n"
    "ds_read_b128 v[160:163], v1 offset:6144\n"
    "v_mul_f32 v70, v26, v34\n"
    "v_mfma_f32_16x16x32_bf16 v[28:31], a[104:107], v[144:147], v[28:31]\n"
    "s_waitcnt vmcnt(1)\n"
    "v_add_u32 v16, v16, v11\n"
    "v_mul_f32 v59, v27, v27\n"
    "v_mfma_f32_16x16x32_bf16 v[20:23], a[36:39], v[148:151], v[20:23]\n"
    "ds_read_b128 v[164:167], v2 offset:6144\n"
    "v_cmp_gt_i32 s[92:93], s68, v16\n"
    "v_mfma_f32_16x16x32_bf16 v[28:31], a[108:111], v[148:151], v[28:31]\n"
    "s_waitcnt lgkmcnt(2)\n"
    "v_cmp_gt_i32 vcc, s69, v16\n"
    "v_fmaak_f32 v59, v18, v59, 0xc0135761\n"
    "v_mfma_f32_16x16x32_bf16 v[20:23], a[40:43], v[152:155], v[20:23]\n"
    "ds_read_b128 v[168:171], v1 offset:7168\n"
    "v_mul_f32 v59, v59, v27\n"
    "v_mfma_f32_16x16x32_bf16 v[28:31], a[112:115], v[152:155], v[28:31]\n"
    "s_cmp_eq_u32 vcc_hi, 0\n"
    "v_exp_f32 v58, v58\n"
    "v_mfma_f32_16x16x32_bf16 v[20:23], a[44:47], v[156:159], v[20:23]\n"
    "s_cselect_b32 s93, 1, 2\n"
    "ds_read_b128 v[172:175], v2 offset:7168\n"
    "v_add_f32 v58, 1.0, v58\n"
    "v_mfma_f32_16x16x32_bf16 v[28:31], a[116:119], v[156:159], v[28:31]\n"
    "s_waitcnt lgkmcnt(2)\n"
    "v_rcp_f32 v58, v58\n"
    "v_mfma_f32_16x16x32_bf16 v[20:23], a[48:51], v[160:163], v[20:23]\n"
    "s_cmp_eq_u32 s92, 0\n"
    "ds_read_b128 v[176:179], v1 offset:8192\n"
    "v_mul_f32 v71, v27, v35\n"
    "v_mfma_f32_16x16x32_bf16 v[28:31], a[120:123], v[160:163], v[28:31]\n"
    "s_cselect_b32 s87, 0, s93\n"
    "v_mov_b32 v17, s87\n"
    "v_mul_f32 v68, v68, v56\n"
    "v_mfma_f32_16x16x32_bf16 v[20:23], a[52:55], v[164:167], v[20:23]\n"
    "ds_read_b128 v[180:183], v2 offset:8192\n"
    "ds_write_b32 v12, v17\n"
    "v_mfma_f32_16x16x32_bf16 v[28:31], a[124:127], v[164:167], v[28:31]\n"
    "s_waitcnt lgkmcnt(3)\n"
    "v_exp_f32 v59, v59\n"
    "v_mfma_f32_16x16x32_bf16 v[20:23], a[56:59], v[168:171], v[20:23]\n"
    "v_add_f32 v59, 1.0, v59\n"
    "v_mul_f32 v69, v69, v57\n"
    "v_mfma_f32_16x16x32_bf16 v[28:31], a[128:131], v[168:171], v[28:31]\n"
    "v_rcp_f32 v59, v59\n"
    "v_mfma_f32_16x16x32_bf16 v[20:23], a[60:63], v[172:175], v[20:23]\n"
    "s_waitcnt lgkmcnt(0)\n"
    "s_barrier\n"
    "ds_read_b32 v16, v10\n"
    "v_mfma_f32_16x16x32_bf16 v[28:31], a[132:135], v[172:175], v[28:31]\n"
    "s_waitcnt lgkmcnt(2)\n"
    "ds_read_b128 v[184:187], v1 offset:18432\n"
    "v_mul_f32 v70, v70, v58\n"
    "v_mfma_f32_16x16x32_bf16 v[20:23], a[64:67], v[176:179], v[20:23]\n"
    "ds_read_b128 v[188:191], v2 offset:18432\n"
    "v_mul_f32 v71, v71, v59\n"
    "v_mfma_f32_16x16x32_bf16 v[28:31], a[136:139], v[176:179], v[28:31]\n"
    "ds_read_b128 v[192:195], v1 offset:19456\n"
    "v_cvt_pk_bf16_f32 v46, v68, v69\n"
    "v_mfma_f32_16x16x32_bf16 v[20:23], a[68:71], v[180:183], v[20:23]\n"
    "s_waitcnt lgkmcnt(3)\n"
    "v_readfirstlane_b32 s71, v16\n"
    "v_cvt_pk_bf16_f32 v47, v70, v71\n"
    "v_mfma_f32_16x16x32_bf16 v[28:31], a[140:143], v[180:183], v[28:31]\n"
    "s_cmp_lg_u32 s71, 2\n"
    "ds_read_b128 v[196:199], v2 offset:19456\n"
    "v_permlane16_swap_b32 v44, v46\n"
    "v_mfma_f32_16x16x32_bf16 v[36:39], a[144:147], v[112:115], 0\n"
    "s_waitcnt lgkmcnt(2)\n"
    "v_permlane16_swap_b32 v45, v47\n"
    "v_mfma_f32_16x16x32_bf16 v[40:43], a[144:147], v[184:187], 0\n"
    "s_cselect_b32 s42, s38, 0\n"
    "ds_read_b128 v[200:203], v1 offset:20480\n"
    "buffer_store_dwordx4 v[44:47], v4, s[44:47], s81 offen sc1\n"
    "v_mfma_f32_16x16x32_bf16 v[36:39], a[148:151], v[116:119], v[36:39]\n"
    "s_cmp_eq_u32 s71, 1\n"
    "v_mul_f32 v52, v20, v20\n"
    "v_fmaak_f32 v52, v18, v52, 0xc0135761\n"
    "v_mfma_f32_16x16x32_bf16 v[40:43], a[148:151], v[188:191], v[40:43]\n"
    "s_cselect_b32 s80, s69, s68\n"
    "ds_read_b128 v[204:207], v2 offset:20480\n"
    "v_mul_f32 v52, v52, v20\n"
    "v_mfma_f32_16x16x32_bf16 v[36:39], a[152:155], v[120:123], v[36:39]\n"
    "s_waitcnt lgkmcnt(2)\n"
    "v_exp_f32 v52, v52\n"
    "v_mfma_f32_16x16x32_bf16 v[40:43], a[152:155], v[192:195], v[40:43]\n"
    "s_cselect_b32 s86, s55, s54\n"
    "ds_read_b128 v[208:211], v1 offset:21504\n"
    "v_mul_f32 v64, v20, v28\n"
    "v_mfma_f32_16x16x32_bf16 v[36:39], a[156:159], v[124:127], v[36:39]\n"
    "s_cselect_b32 s78, s57, s56\n"
    "v_add_f32 v52, 1.0, v52\n"
    "v_mul_f32 v53, v21, v21\n"
    "v_mfma_f32_16x16x32_bf16 v[40:43], a[156:159], v[196:199], v[40:43]\n"
    "s_mul_i32 s87, s80, s58\n"
    "ds_read_b128 v[212:215], v2 offset:21504\n"
    "v_fmaak_f32 v53, v18, v53, 0xc0135761\n"
    "v_mfma_f32_16x16x32_bf16 v[36:39], a[160:163], v[128:131], v[36:39]\n"
    "s_waitcnt lgkmcnt(2)\n"
    "v_rcp_f32 v52, v52\n"
    "v_mfma_f32_16x16x32_bf16 v[40:43], a[160:163], v[200:203], v[40:43]\n"
    "s_add_i32 s86, s86, s87\n"
    "ds_read_b128 v[216:219], v1 offset:22528\n"
    "v_mul_f32 v53, v53, v21\n"
    "v_mfma_f32_16x16x32_bf16 v[36:39], a[164:167], v[132:135], v[36:39]\n"
    "s_add_i32 m0, s66, 36864\n"
    "v_exp_f32 v53, v53\n"
    "v_mfma_f32_16x16x32_bf16 v[40:43], a[164:167], v[204:207], v[40:43]\n"
    "ds_read_b128 v[220:223], v2 offset:22528\n"
    "buffer_load_dwordx4 v0, s[40:43], s86 offen offset:0 sc1 lds\n"
    "v_mfma_f32_16x16x32_bf16 v[36:39], a[168:171], v[136:139], v[36:39]\n"
    "s_waitcnt lgkmcnt(2)\n"
    "v_mul_f32 v65, v21, v29\n"
    "v_add_f32 v53, 1.0, v53\n"
    "v_mfma_f32_16x16x32_bf16 v[40:43], a[168:171], v[208:211], v[40:43]\n"
    "s_and_b32 s87, s80, 7\n"
    "ds_read_b128 v[224:227], v1 offset:23552\n"
    "v_mul_f32 v54, v22, v22\n"
    "v_mfma_f32_16x16x32_bf16 v[36:39], a[172:175], v[140:143], v[36:39]\n"
    "s_add_i32 m0, s66, 37760\n"
    "v_rcp_f32 v53, v53\n"
    "v_mfma_f32_16x16x32_bf16 v[40:43], a[172:175], v[212:215], v[40:43]\n"
    "s_mul_i32 s87, s87, s59\n"
    "ds_read_b128 v[228:231], v2 offset:23552\n"
    "buffer_load_dwordx4 v0, s[40:43], s86 offen offset:128 sc1 lds\n"
    "v_mfma_f32_16x16x32_bf16 v[36:39], a[176:179], v[144:147], v[36:39]\n"
    "s_waitcnt lgkmcnt(2)\n"
    "v_fmaak_f32 v54, v18, v54, 0xc0135761\n"
    "v_mul_f32 v54, v54, v22\n"
    "v_mfma_f32_16x16x32_bf16 v[40:43], a[176:179], v[216:219], v[40:43]\n"
    "s_add_i32 s78, s78, s87\n"
    "ds_read_b128 v[232:235], v1 offset:24576\n"
    "v_mul_f32 v66, v22, v30\n"
    "v_mfma_f32_16x16x32_bf16 v[36:39], a[180:183], v[148:151], v[36:39]\n"
    "s_add_i32 m0, s66, 38656\n"
    "v_exp_f32 v54, v54\n"
    "v_mfma_f32_16x16x32_bf16 v[40:43], a[180:183], v[220:223], v[40:43]\n"
    "s_and_b32 s92, s71, 1\n"
    "ds_read_b128 v[236:239], v2 offset:24576\n"
    "buffer_load_dwordx4 v0, s[40:43], s86 offen offset:256 sc1 lds\n"
    "v_mfma_f32_16x16x32_bf16 v[36:39], a[184:187], v[152:155], v[36:39]\n"
    "s_waitcnt lgkmcnt(2)\n"
    "v_add_f32 v54, 1.0, v54\n"
    "v_mul_f32 v55, v23, v23\n"
    "v_mfma_f32_16x16x32_bf16 v[40:43], a[184:187], v[224:227], v[40:43]\n"
    "s_add_i32 s69, s69, s92\n"
    "ds_read_b128 v[240:243], v1 offset:25600\n"
    "v_fmaak_f32 v55, v18, v55, 0xc0135761\n"
    "v_mfma_f32_16x16x32_bf16 v[36:39], a[188:191], v[156:159], v[36:39]\n"
    "s_add_i32 m0, s66, 39552\n"
    "v_rcp_f32 v54, v54\n"
    "v_mfma_f32_16x16x32_bf16 v[40:43], a[188:191], v[228:231], v[40:43]\n"
    "s_lshr_b32 s92, 1, s71\n"
    "ds_read_b128 v[244:247], v2 offset:25600\n"
    "buffer_load_dwordx4 v0, s[40:43], s86 offen offset:384 sc1 lds\n"
    "v_mfma_f32_16x16x32_bf16 v[36:39], a[192:195], v[160:163], v[36:39]\n"
    "s_waitcnt lgkmcnt(2)\n"
    "v_mul_f32 v55, v55, v23\n"
    "v_mul_f32 v67, v23, v31\n"
    "v_mfma_f32_16x16x32_bf16 v[40:43], a[192:195], v[232:235], v[40:43]\n"
    "s_add_i32 s68, s68, s92\n"
    "ds_read_b128 v[248:251], v1 offset:26624\n"
    "v_mul_f32 v64, v64, v52\n"
    "v_mfma_f32_16x16x32_bf16 v[36:39], a[196:199], v[164:167], v[36:39]\n"
    "s_add_i32 m0, s66, 40448\n"
    "v_exp_f32 v55, v55\n"
    "v_mfma_f32_16x16x32_bf16 v[40:43], a[196:199], v[236:239], v[40:43]\n"
    "ds_read_b128 v[252:255], v2 offset:26624\n"
    "buffer_load_dwordx4 v0, s[40:43], s86 offen offset:512 sc1 lds\n"
    "v_mfma_f32_16x16x32_bf16 v[36:39], a[200:203], v[168:171], v[36:39]\n"
    "s_waitcnt lgkmcnt(2)\n"
    "v_add_f32 v55, 1.0, v55\n"
    "v_mul_f32 v65, v65, v53\n"
    "v_mfma_f32_16x16x32_bf16 v[40:43], a[200:203], v[240:243], v[40:43]\n"
    "v_rcp_f32 v55, v55\n"
    "v_mfma_f32_16x16x32_bf16 v[36:39], a[204:207], v[172:175], v[36:39]\n"
    "v_mul_f32 v66, v66, v54\n"
    "v_mul_f32 v67, v67, v55\n"
    "v_mfma_f32_16x16x32_bf16 v[40:43], a[204:207], v[244:247], v[40:43]\n"
    "v_cvt_pk_bf16_f32 v44, v64, v65\n"
    "v_cvt_pk_bf16_f32 v45, v66, v67\n"
    "v_mfma_f32_16x16x32_bf16 v[36:39], a[208:211], v[176:179], v[36:39]\n"
    "s_waitcnt lgkmcnt(0)\n"
    "v_mfma_f32_16x16x32_bf16 v[40:43], a[208:211], v[248:251], v[40:43]\n"
    "s_add_i32 m0, s66, 55296\n"
    "v_mfma_f32_16x16x32_bf16 v[36:39], a[212:215], v[180:183], v[36:39]\n"
    "buffer_load_dwordx4 v6, s[40:43], s86 offen offset:0 sc1 lds\n"
    "v_mfma_f32_16x16x32_bf16 v[40:43], a[212:215], v[252:255], v[40:43]\n"
    "v_mfma_f32_16x16x32_bf16 v[24:27], a[0:3], v[184:187], 0\n"
    "s_add_i32 m0, s66, 56192\n"
    "v_mfma_f32_16x16x32_bf16 v[32:35], a[72:75], v[184:187], 0\n"
    "buffer_load_dwordx4 v6, s[40:43], s86 offen offset:128 sc1 lds\n"
    "v_mfma_f32_16x16x32_bf16 v[24:27], a[4:7], v[188:191], v[24:27]\n"
    "v_permlane32_swap_b32 v36, v40\n"
    "v_mfma_f32_16x16x32_bf16 v[32:35], a[76:79], v[188:191], v[32:35]\n"
    "v_permlane32_swap_b32 v37, v41\n"
    "v_mfma_f32_16x16x32_bf16 v[24:27], a[8:11], v[192:195], v[24:27]\n"
    "s_add_i32 m0, s66, 57088\n"
    "v_permlane32_swap_b32 v38, v42\n"
    "v_mfma_f32_16x16x32_bf16 v[32:35], a[80:83], v[192:195], v[32:35]\n"
    "buffer_load_dwordx4 v6, s[40:43], s86 offen offset:256 sc1 lds\n"
    "v_mul_f32 v60, v36, v36\n"
    "v_mfma_f32_16x16x32_bf16 v[24:27], a[12:15], v[196:199], v[24:27]\n"
    "v_permlane32_swap_b32 v39, v43\n"
    "v_mfma_f32_16x16x32_bf16 v[32:35], a[84:87], v[196:199], v[32:35]\n"
    "v_fmaak_f32 v60, v18, v60, 0xc0135761\n"
    "v_mul_f32 v60, v60, v36\n"
    "v_mfma_f32_16x16x32_bf16 v[24:27], a[16:19], v[200:203], v[24:27]\n"
    "s_add_i32 m0, s66, 57984\n"
    "v_exp_f32 v60, v60\n"
    "v_mfma_f32_16x16x32_bf16 v[32:35], a[88:91], v[200:203], v[32:35]\n"
    "s_waitcnt vmcnt(8)\n"
    "s_barrier\n"
    "buffer_load_dwordx4 v6, s[40:43], s86 offen offset:384 sc1 lds\n"
    "v_mul_f32 v72, v36, v40\n"
    "v_mfma_f32_16x16x32_bf16 v[24:27], a[20:23], v[204:207], v[24:27]\n"
    "s_mov_b64 exec, s[90:91]\n"
    "buffer_store_dword v19, off, s[44:47], s82 sc1\n"
    "s_mov_b64 exec, -1\n"
    "v_add_f32 v60, 1.0, v60\n"
    "v_mul_f32 v61, v37, v37\n"
    "v_mfma_f32_16x16x32_bf16 v[32:35], a[92:95], v[204:207], v[32:35]\n"
    "s_mov_b32 s46, s38\n"
    "v_rcp_f32 v60, v60\n"
    "v_mfma_f32_16x16x32_bf16 v[24:27], a[24:27], v[208:211], v[24:27]\n"
    "s_add_i32 m0, s66, 58880\n"
    "v_fmaak_f32 v61, v18, v61, 0xc0135761\n"
    "v_mul_f32 v61, v61, v37\n"
    "v_mfma_f32_16x16x32_bf16 v[32:35], a[96:99], v[208:211], v[32:35]\n"
    "s_mov_b32 s81, s75\n"
    "buffer_load_dwordx4 v6, s[40:43], s86 offen offset:512 sc1 lds\n"
    "v_mul_f32 v73, v37, v41\n"
    "v_mfma_f32_16x16x32_bf16 v[24:27], a[28:31], v[212:215], v[24:27]\n"
    "s_mov_b32 s82, s76\n"
    "v_exp_f32 v61, v61\n"
    "v_mfma_f32_16x16x32_bf16 v[32:35], a[100:103], v[212:215], v[32:35]\n"
    "s_mov_b32 s83, s77\n"
    "v_add_f32 v61, 1.0, v61\n"
    "v_mul_f32 v62, v38, v38\n"
    "v_mfma_f32_16x16x32_bf16 v[24:27], a[32:35], v[216:219], v[24:27]\n"
    "s_mov_b32 s75, s78\n"
    "v_rcp_f32 v61, v61\n"
    "v_mfma_f32_16x16x32_bf16 v[32:35], a[104:107], v[216:219], v[32:35]\n"
    "s_mov_b32 s77, s80\n"
    "v_fmaak_f32 v62, v18, v62, 0xc0135761\n"
    "v_mul_f32 v62, v62, v38\n"
    "v_mfma_f32_16x16x32_bf16 v[24:27], a[36:39], v[220:223], v[24:27]\n"
    "s_mul_i32 s76, s71, s94\n"
    "v_exp_f32 v62, v62\n"
    "v_mfma_f32_16x16x32_bf16 v[32:35], a[108:111], v[220:223], v[32:35]\n"
    "s_add_i32 s76, s76, s60\n"
    "v_mul_f32 v74, v38, v42\n"
    "v_add_f32 v62, 1.0, v62\n"
    "v_mfma_f32_16x16x32_bf16 v[24:27], a[40:43], v[224:227], v[24:27]\n"
    "v_rcp_f32 v62, v62\n"
    "v_mfma_f32_16x16x32_bf16 v[32:35], a[112:115], v[224:227], v[32:35]\n"
    "v_mul_f32 v63, v39, v39\n"
    "v_fmaak_f32 v63, v18, v63, 0xc0135761\n"
    "v_mfma_f32_16x16x32_bf16 v[24:27], a[44:47], v[228:231], v[24:27]\n"
    "v_mul_f32 v63, v63, v39\n"
    "v_mul_f32 v75, v39, v43\n"
    "v_mfma_f32_16x16x32_bf16 v[32:35], a[116:119], v[228:231], v[32:35]\n"
    "v_exp_f32 v63, v63\n"
    "v_mfma_f32_16x16x32_bf16 v[24:27], a[48:51], v[232:235], v[24:27]\n"
    "global_load_dword v16, v3, s[52:53] sc1\n"
    "v_add_f32 v63, 1.0, v63\n"
    "v_mfma_f32_16x16x32_bf16 v[32:35], a[120:123], v[232:235], v[32:35]\n"
    "v_rcp_f32 v63, v63\n"
    "v_mfma_f32_16x16x32_bf16 v[24:27], a[52:55], v[236:239], v[24:27]\n"
    "v_mul_f32 v72, v72, v60\n"
    "v_mul_f32 v73, v73, v61\n"
    "v_mfma_f32_16x16x32_bf16 v[32:35], a[124:127], v[236:239], v[32:35]\n"
    "v_mul_f32 v74, v74, v62\n"
    "v_mul_f32 v75, v75, v63\n"
    "v_mfma_f32_16x16x32_bf16 v[24:27], a[56:59], v[240:243], v[24:27]\n"
    "v_cvt_pk_bf16_f32 v48, v72, v73\n"
    "v_cvt_pk_bf16_f32 v50, v72, v73\n"
    "v_mfma_f32_16x16x32_bf16 v[32:35], a[128:131], v[240:243], v[32:35]\n"
    "v_cvt_pk_bf16_f32 v49, v74, v75\n"
    "v_cvt_pk_bf16_f32 v51, v74, v75\n"
    "v_mfma_f32_16x16x32_bf16 v[24:27], a[60:63], v[244:247], v[24:27]\n"
    "v_permlane16_swap_b32 v48, v50\n"
    "v_mfma_f32_16x16x32_bf16 v[32:35], a[132:135], v[244:247], v[32:35]\n"
    "s_waitcnt vmcnt(6) lgkmcnt(0)\n"
    "s_barrier\n"
    "ds_read_b128 v[112:115], v8 offset:0\n"
    "v_mfma_f32_16x16x32_bf16 v[24:27], a[64:67], v[248:251], v[24:27]\n"
    "ds_read_b128 v[116:119], v9 offset:0\n"
    "ds_read_b128 v[120:123], v8 offset:1024\n"
    "v_mfma_f32_16x16x32_bf16 v[32:35], a[136:139], v[248:251], v[32:35]\n"
    "ds_read_b128 v[124:127], v9 offset:1024\n"
    "v_permlane16_swap_b32 v49, v51\n"
    "v_mfma_f32_16x16x32_bf16 v[24:27], a[68:71], v[252:255], v[24:27]\n"
    "v_mfma_f32_16x16x32_bf16 v[32:35], a[140:143], v[252:255], v[32:35]\n"
    "s_cmp_eq_u32 s42, 0\n"
    "s_cbranch_scc1 L_idle1%=\n"
    "L_body1%=:\n"
    "s_waitcnt lgkmcnt(2)\n"
    "v_mfma_f32_16x16x32_bf16 v[20:23], a[0:3], v[112:115], 0\n"
    "ds_read_b128 v[128:131], v8 offset:2048\n"
    "v_mov_b32 v19, s83\n"
    "v_mfma_f32_16x16x32_bf16 v[28:31], a[72:75], v[112:115], 0\n"
    "buffer_store_dwordx4 v[48:51], v5, s[44:47], s81 offen sc1\n"
    "v_mfma_f32_16x16x32_bf16 v[20:23], a[4:7], v[116:119], v[20:23]\n"
    "ds_read_b128 v[132:135], v9 offset:2048\n"
    "v_mul_f32 v56, v24, v24\n"
    "v_mfma_f32_16x16x32_bf16 v[28:31], a[76:79], v[116:119], v[28:31]\n"
    "s_waitcnt lgkmcnt(2)\n"
    "v_fmaak_f32 v56, v18, v56, 0xc0135761\n"
    "v_mul_f32 v56, v56, v24\n"
    "v_mfma_f32_16x16x32_bf16 v[20:23], a[8:11], v[120:123], v[20:23]\n"
    "ds_read_b128 v[136:139], v8 offset:3072\n"
    "v_mul_f32 v68, v24, v32\n"
    "v_mfma_f32_16x16x32_bf16 v[28:31], a[80:83], v[120:123], v[28:31]\n"
    "v_exp_f32 v56, v56\n"
    "v_mfma_f32_16x16x32_bf16 v[20:23], a[12:15], v[124:127], v[20:23]\n"
    "ds_read_b128 v[140:143], v9 offset:3072\n"
    "v_add_f32 v56, 1.0, v56\n"
    "v_mfma_f32_16x16x32_bf16 v[28:31], a[84:87], v[124:127], v[28:31]\n"
    "s_waitcnt lgkmcnt(2)\n"
    "v_rcp_f32 v56, v56\n"
    "v_mfma_f32_16x16x32_bf16 v[20:23], a[16:19], v[128:131], v[20:23]\n"
    "ds_read_b128 v[144:147], v8 offset:4096\n"
    "v_mul_f32 v57, v25, v25\n"
    "v_mfma_f32_16x16x32_bf16 v[28:31], a[88:91], v[128:131], v[28:31]\n"
    "v_fmaak_f32 v57, v18, v57, 0xc0135761\n"
    "v_mul_f32 v57, v57, v25\n"
    "v_mfma_f32_16x16x32_bf16 v[20:23], a[20:23], v[132:135], v[20:23]\n"
    "ds_read_b128 v[148:151], v9 offset:4096\n"
    "v_mul_f32 v69, v25, v33\n"
    "v_mfma_f32_16x16x32_bf16 v[28:31], a[92:95], v[132:135], v[28:31]\n"
    "s_waitcnt lgkmcnt(2)\n"
    "v_exp_f32 v57, v57\n"
    "v_mfma_f32_16x16x32_bf16 v[20:23], a[24:27], v[136:139], v[20:23]\n"
    "ds_read_b128 v[152:155], v8 offset:5120\n"
    "v_add_f32 v57, 1.0, v57\n"
    "v_mfma_f32_16x16x32_bf16 v[28:31], a[96:99], v[136:139], v[28:31]\n"
    "v_rcp_f32 v57, v57\n"
    "v_mfma_f32_16x16x32_bf16 v[20:23], a[28:31], v[140:143], v[20:23]\n"
    "ds_read_b128 v[156:159], v9 offset:5120\n"
    "v_mul_f32 v58, v26, v26\n"
    "v_mfma_f32_16x16x32_bf16 v[28:31], a[100:103], v[140:143], v[28:31]\n"
    "s_waitcnt lgkmcnt(2)\n"
    "v_fmaak_f32 v58, v18, v58, 0xc0135761\n"
    "v_mul_f32 v58, v58, v26\n"
    "v_mfma_f32_16x16x32_bf16 v[20:23], a[32:35], v[144:147], v[20:23]\n"
    "ds_read_b128 v[160:163], v8 offset:6144\n"
    "v_mul_f32 v70, v26, v34\n"
    "v_mfma_f32_16x16x32_bf16 v[28:31], a[104:107], v[144:147], v[28:31]\n"
    "s_waitcnt vmcnt(1)\n"
    "v_add_u32 v16, v16, v11\n"
    "v_mul_f32 v59, v27, v27\n"
    "v_mfma_f32_16x16x32_bf16 v[20:23], a[36:39], v[148:151], v[20:23]\n"
    "ds_read_b128 v[164:167], v9 offset:6144\n"
    "v_cmp_gt_i32 s[92:93], s68, v16\n"
    "v_mfma_f32_16x16x32_bf16 v[28:31], a[108:111], v[148:151], v[28:31]\n"
    "s_waitcnt lgkmcnt(2)\n"
    "v_cmp_gt_i32 vcc, s69, v16\n"
    "v_fmaak_f32 v59, v18, v59, 0xc0135761\n"
    "v_mfma_f32_16x16x32_bf16 v[20:23], a[40:43], v[152:155], v[20:23]\n"
    "ds_read_b128 v[168:171], v8 offset:7168\n"
    "v_mul_f32 v59, v59, v27\n"
    "v_mfma_f32_16x16x32_bf16 v[28:31], a[112:115], v[152:155], v[28:31]\n"
    "s_cmp_eq_u32 vcc_hi, 0\n"
    "v_exp_f32 v58, v58\n"
    "v_mfma_f32_16x16x32_bf16 v[20:23], a[44:47], v[156:159], v[20:23]\n"
    "s_cselect_b32 s93, 1, 2\n"
    "ds_read_b128 v[172:175], v9 offset:7168\n"
    "v_add_f32 v58, 1.0, v58\n"
    "v_mfma_f32_16x16x32_bf16 v[28:31], a[116:119], v[156:159], v[28:31]\n"
    "s_waitcnt lgkmcnt(2)\n"
    "v_rcp_f32 v58, v58\n"
    "v_mfma_f32_16x16x32_bf16 v[20:23], a[48:51], v[160:163], v[20:23]\n"
    "s_cmp_eq_u32 s92, 0\n"
    "ds_read_b128 v[176:179], v8 offset:8192\n"
    "v_mul_f32 v71, v27, v35\n"
    "v_mfma_f32_16x16x32_bf16 v[28:31], a[120:123], v[160:163], v[28:31]\n"
    "s_cselect_b32 s87, 0, s93\n"
    "v_mov_b32 v17, s87\n"
    "v_mul_f32 v68, v68, v56\n"
    "v_mfma_f32_16x16x32_bf16 v[20:23], a[52:55], v[164:167], v[20:23]\n"
    "ds_read_b128 v[180:183], v9 offset:8192\n"
    "ds_write_b32 v12, v17\n"
    "v_mfma_f32_16x16x32_bf16 v[28:31], a[124:127], v[164:167], v[28:31]\n"
    "s_waitcnt lgkmcnt(3)\n"
    "v_exp_f32 v59, v59\n"
    "v_mfma_f32_16x16x32_bf16 v[20:23], a[56:59], v[168:171], v[20:23]\n"
    "v_add_f32 v59, 1.0, v59\n"
    "v_mul_f32 v69, v69, v57\n"
    "v_mfma_f32_16x16x32_bf16 v[28:31], a[128:131], v[168:171], v[28:31]\n"
    "v_rcp_f32 v59, v59\n"
    "v_mfma_f32_16x16x32_bf16 v[20:23], a[60:63], v[172:175], v[20:23]\n"
    "s_waitcnt lgkmcnt(0)\n"
    "s_barrier\n"
    "ds_read_b32 v16, v10\n"
    "v_mfma_f32_16x16x32_bf16 v[28:31], a[132:135], v[172:175], v[28:31]\n"
    "s_waitcnt lgkmcnt(2)\n"
    "ds_read_b128 v[184:187], v8 offset:18432\n"
    "v_mul_f32 v70, v70, v58\n"
    "v_mfma_f32_16x16x32_bf16 v[20:23], a[64:67], v[176:179], v[20:23]\n"
    "ds_read_b128 v[188:191], v9 offset:18432\n"
    "v_mul_f32 v71, v71, v59\n"
    "v_mfma_f32_16x16x32_bf16 v[28:31], a[136:139], v[176:179], v[28:31]\n"
    "ds_read_b128 v[192:195], v8 offset:19456\n"
    "v_cvt_pk_bf16_f32 v46, v68, v69\n"
    "v_mfma_f32_16x16x32_bf16 v[20:23], a[68:71], v[180:183], v[20:23]\n"
    "s_waitcnt lgkmcnt(3)\n"
    "v_readfirstlane_b32 s71, v16\n"
    "v_cvt_pk_bf16_f32 v47, v70, v71\n"
    "v_mfma_f32_16x16x32_bf16 v[28:31], a[140:143], v[180:183], v[28:31]\n"
    "s_cmp_lg_u32 s71, 2\n"
    "ds_read_b128 v[196:199], v9 offset:19456\n"
    "v_permlane16_swap_b32 v44, v46\n"
    "v_mfma_f32_16x16x32_bf16 v[36:39], a[144:147], v[112:115], 0\n"
    "s_waitcnt lgkmcnt(2)\n"
    "v_permlane16_swap_b32 v45, v47\n"
    "v_mfma_f32_16x16x32_bf16 v[40:43], a[144:147], v[184:187], 0\n"
    "s_cselect_b32 s42, s38, 0\n"
    "ds_read_b128 v[200:203], v8 offset:20480\n"
    "buffer_store_dwordx4 v[44:47], v4, s[44:47], s81 offen sc1\n"
    "v_mfma_f32_16x16x32_bf16 v[36:39], a[148:151], v[116:119], v[36:39]\n"
    "s_cmp_eq_u32 s71, 1\n"
    "v_mul_f32 v52, v20, v20\n"
    "v_fmaak_f32 v52, v18, v52, 0xc0135761\n"
    "v_mfma_f32_16x16x32_bf16 v[40:43], a[148:151], v[188:191], v[40:43]\n"
    "s_cselect_b32 s80, s69, s68\n"
    "ds_read_b128 v[204:207], v9 offset:20480\n"
    "v_mul_f32 v52, v52, v20\n"
    "v_mfma_f32_16x16x32_bf16 v[36:39], a[152:155], v[120:123], v[36:39]\n"
    "s_waitcnt lgkmcnt(2)\n"
    "v_exp_f32 v52, v52\n"
    "v_mfma_f32_16x16x32_bf16 v[40:43], a[152:155], v[192:195], v[40:43]\n"
    "s_cselect_b32 s86, s55, s54\n"
    "ds_read_b128 v[208:211], v8 offset:21504\n"
    "v_mul_f32 v64, v20, v28\n"
    "v_mfma_f32_16x16x32_bf16 v[36:39], a[156:159], v[124:127], v[36:39]\n"
    "s_cselect_b32 s78, s57, s56\n"
    "v_add_f32 v52, 1.0, v52\n"
    "v_mul_f32 v53, v21, v21\n"
    "v_mfma_f32_16x16x32_bf16 v[40:43], a[156:159], v[196:199], v[40:43]\n"
    "s_mul_i32 s87, s80, s58\n"
    "ds_read_b128 v[212:215], v9 offset:21504\n"
    "v_fmaak_f32 v53, v18, v53, 0xc0135761\n"
    "v_mfma_f32_16x16x32_bf16 v[36:39], a[160:163], v[128:131], v[36:39]\n"
    "s_waitcnt lgkmcnt(2)\n"
    "v_rcp_f32 v52, v52\n"
    "v_mfma_f32_16x16x32_bf16 v[40:43], a[160:163], v[200:203], v[40:43]\n"
    "s_add_i32 s86, s86, s87\n"
    "ds_read_b128 v[216:219], v8 offset:22528\n"
    "v_mul_f32 v53, v53, v21\n"
    "v_mfma_f32_16x16x32_bf16 v[36:39], a[164:167], v[132:135], v[36:39]\n"
    "s_add_i32 m0, s66, 0\n"
    "v_exp_f32 v53, v53\n"
    "v_mfma_f32_16x16x32_bf16 v[40:43], a[164:167], v[204:207], v[40:43]\n"
    "ds_read_b128 v[220:223], v9 offset:22528\n"
    "buffer_load_dwordx4 v0, s[40:43], s86 offen offset:0 sc1 lds\n"
    "v_mfma_f32_16x16x32_bf16 v[36:39], a[168:171], v[136:139], v[36:39]\n"
    "s_waitcnt lgkmcnt(2)\n"
    "v_mul_f32 v65, v21, v29\n"
    "v_add_f32 v53, 1.0, v53\n"
    "v_mfma_f32_16x16x32_bf16 v[40:43], a[168:171], v[208:211], v[40:43]\n"
    "s_and_b32 s87, s80, 7\n"
    "ds_read_b128 v[224:227], v8 offset:23552\n"
    "v_mul_f32 v54, v22, v22\n"
    "v_mfma_f32_16x16x32_bf16 v[36:39], a[172:175], v[140:143], v[36:39]\n"
    "s_add_i32 m0, s66, 896\n"
    "v_rcp_f32 v53, v53\n"
    "v_mfma_f32_16x16x32_bf16 v[40:43], a[172:175], v[212:215], v[40:43]\n"
    "s_mul_i32 s87, s87, s59\n"
    "ds_read_b128 v[228:231], v9 offset:23552\n"
    "buffer_load_dwordx4 v0, s[40:43], s86 offen offset:128 sc1 lds\n"
    "v_mfma_f32_16x16x32_bf16 v[36:39], a[176:179], v[144:147], v[36:39]\n"
    "s_waitcnt lgkmcnt(2)\n"
    "v_fmaak_f32 v54, v18, v54, 0xc0135761\n"
    "v_mul_f32 v54, v54, v22\n"
    "v_mfma_f32_16x16x32_bf16 v[40:43], a[176:179], v[216:219], v[40:43]\n"
    "s_add_i32 s78, s78, s87\n"
    "ds_read_b128 v[232:235], v8 offset:24576\n"
    "v_mul_f32 v66, v22, v30\n"
    "v_mfma_f32_16x16x32_bf16 v[36:39], a[180:183], v[148:151], v[36:39]\n"
    "s_add_i32 m0, s66, 1792\n"
    "v_exp_f32 v54, v54\n"
    "v_mfma_f32_16x16x32_bf16 v[40:43], a[180:183], v[220:223], v[40:43]\n"
    "s_and_b32 s92, s71, 1\n"
    "ds_read_b128 v[236:239], v9 offset:24576\n"
    "buffer_load_dwordx4 v0, s[40:43], s86 offen offset:256 sc1 lds\n"
    "v_mfma_f32_16x16x32_bf16 v[36:39], a[184:187], v[152:155], v[36:39]\n"
    "s_waitcnt lgkmcnt(2)\n"
    "v_add_f32 v54, 1.0, v54\n"
    "v_mul_f32 v55, v23, v23\n"
    "v_mfma_f32_16x16x32_bf16 v[40:43], a[184:187], v[224:227], v[40:43]\n"
    "s_add_i32 s69, s69, s92\n"
    "ds_read_b128 v[240:243], v8 offset:25600\n"
    "v_fmaak_f32 v55, v18, v55, 0xc0135761\n"
    "v_mfma_f32_16x16x32_bf16 v[36:39], a[188:191], v[156:159], v[36:39]\n"
    "s_add_i32 m0, s66, 2688\n"
    "v_rcp_f32 v54, v54\n"
    "v_mfma_f32_16x16x32_bf16 v[40:43], a[188:191], v[228:231], v[40:43]\n"
    "s_lshr_b32 s92, 1, s71\n"
    "ds_read_b128 v[244:247], v9 offset:25600\n"
    "buffer_load_dwordx4 v0, s[40:43], s86 offen offset:384 sc1 lds\n"
    "v_mfma_f32_16x16x32_bf16 v[36:39], a[192:195], v[160:163], v[36:39]\n"
    "s_waitcnt lgkmcnt(2)\n"
    "v_mul_f32 v55, v55, v23\n"
    "v_mul_f32 v67, v23, v31\n"
    "v_mfma_f32_16x16x32_bf16 v[40:43], a[192:195], v[232:235], v[40:43]\n"
    "s_add_i32 s68, s68, s92\n"
    "ds_read_b128 v[248:251], v8 offset:26624\n"
    "v_mul_f32 v64, v64, v52\n"
    "v_mfma_f32_16x16x32_bf16 v[36:39], a[196:199], v[164:167], v[36:39]\n"
    "s_add_i32 m0, s66, 3584\n"
    "v_exp_f32 v55, v55\n"
    "v_mfma_f32_16x16x32_bf16 v[40:43], a[196:199], v[236:239], v[40:43]\n"
    "ds_read_b128 v[252:255], v9 offset:26624\n"
    "buffer_load_dwordx4 v0, s[40:43], s86 offen offset:512 sc1 lds\n"
    "v_mfma_f32_16x16x32_bf16 v[36:39], a[200:203], v[168:171], v[36:39]\n"
    "s_waitcnt lgkmcnt(2)\n"
    "v_add_f32 v55, 1.0, v55\n"
    "v_mul_f32 v65, v65, v53\n"
    "v_mfma_f32_16x16x32_bf16 v[40:43], a[200:203], v[240:243], v[40:43]\n"
    "v_rcp_f32 v55, v55\n"
    "v_mfma_f32_16x16x32_bf16 v[36:39], a[204:207], v[172:175], v[36:39]\n"
    "v_mul_f32 v66, v66, v54\n"
    "v_mul_f32 v67, v67, v55\n"
    "v_mfma_f32_16x16x32_bf16 v[40:43], a[204:207], v[244:247], v[40:43]\n"
    "v_cvt_pk_bf16_f32 v44, v64, v65\n"
    "v_cvt_pk_bf16_f32 v45, v66, v67\n"
    "v_mfma_f32_16x16x32_bf16 v[36:39], a[208:211], v[176:179], v[36:39]\n"
    "s_waitcnt lgkmcnt(0)\n"
    "v_mfma_f32_16x16x32_bf16 v[40:43], a[208:211], v[248:251], v[40:43]\n"
    "s_add_i32 m0, s66, 18432\n"
    "v_mfma_f32_16x16x32_bf16 v[36:39], a[212:215], v[180:183], v[36:39]\n"
    "buffer_load_dwordx4 v6, s[40:43], s86 offen offset:0 sc1 lds\n"
    "v_mfma_f32_16x16x32_bf16 v[40:43], a[212:215], v[252:255], v[40:43]\n"
    "v_mfma_f32_16x16x32_bf16 v[24:27], a[0:3], v[184:187], 0\n"
    "s_add_i32 m0, s66, 19328\n"
    "v_mfma_f32_16x16x32_bf16 v[32:35], a[72:75], v[184:187], 0\n"
    "buffer_load_dwordx4 v6, s[40:43], s86 offen offset:128 sc1 lds\n"
    "v_mfma_f32_16x16x32_bf16 v[24:27], a[4:7], v[188:191], v[24:27]\n"
    "v_permlane32_swap_b32 v36, v40\n"
    "v_mfma_f32_16x16x32_bf16 v[32:35], a[76:79], v[188:191], v[32:35]\n"
    "v_permlane32_swap_b32 v37, v41\n"
    "v_mfma_f32_16x16x32_bf16 v[24:27], a[8:11], v[192:195], v[24:27]\n"
    "s_add_i32 m0, s66, 20224\n"
    "v_permlane32_swap_b32 v38, v42\n"
    "v_mfma_f32_16x16x32_bf16 v[32:35], a[80:83], v[192:195], v[32:35]\n"
    "buffer_load_dwordx4 v6, s[40:43], s86 offen offset:256 sc1 lds\n"
    "v_mul_f32 v60, v36, v36\n"
    "v_mfma_f32_16x16x32_bf16 v[24:27], a[12:15], v[196:199], v[24:27]\n"
    "v_permlane32_swap_b32 v39, v43\n"
    "v_mfma_f32_16x16x32_bf16 v[32:35], a[84:87], v[196:199], v[32:35]\n"
    "v_fmaak_f32 v60, v18, v60, 0xc0135761\n"
    "v_mul_f32 v60, v60, v36\n"
    "v_mfma_f32_16x16x32_bf16 v[24:27], a[16:19], v[200:203], v[24:27]\n"
    "s_add_i32 m0, s66, 21120\n"
    "v_exp_f32 v60, v60\n"
    "v_mfma_f32_16x16x32_bf16 v[32:35], a[88:91], v[200:203], v[32:35]\n"
    "s_waitcnt vmcnt(8)\n"
    "s_barrier\n"
    "buffer_load_dwordx4 v6, s[40:43], s86 offen offset:384 sc1 lds\n"
    "v_mul_f32 v72, v36, v40\n"
    "v_mfma_f32_16x16x32_bf16 v[24:27], a[20:23], v[204:207], v[24:27]\n"
    "s_mov_b64 exec, s[90:91]\n"
    "buffer_store_dword v19, off, s[44:47], s82 sc1\n"
    "s_mov_b64 exec, -1\n"
    "v_add_f32 v60, 1.0, v60\n"
    "v_mul_f32 v61, v37, v37\n"
    "v_mfma_f32_16x16x32_bf16 v[32:35], a[92:95], v[204:207], v[32:35]\n"
    "s_mov_b32 s46, s38\n"
    "v_rcp_f32 v60, v60\n"
    "v_mfma_f32_16x16x32_bf16 v[24:27], a[24:27], v[208:211], v[24:27]\n"
    "s_add_i32 m0, s66, 22016\n"
    "v_fmaak_f32 v61, v18, v61, 0xc0135761\n"
    "v_mul_f32 v61, v61, v37\n"
    "v_mfma_f32_16x16x32_bf16 v[32:35], a[96:99], v[208:211], v[32:35]\n"
    "s_mov_b32 s81, s75\n"
    "buffer_load_dwordx4 v6, s[40:43], s86 offen offset:512 sc1 lds\n"
    "v_mul_f32 v73, v37, v41\n"
    "v_mfma_f32_16x16x32_bf16 v[24:27], a[28:31], v[212:215], v[24:27]\n"
    "s_mov_b32 s82, s76\n"
    "v_exp_f32 v61, v61\n"
    "v_mfma_f32_16x16x32_bf16 v[32:35], a[100:103], v[212:215], v[32:35]\n"
    "s_mov_b32 s83, s77\n"
    "v_add_f32 v61, 1.0, v61\n"
    "v_mul_f32 v62, v38, v38\n"
    "v_mfma_f32_16x16x32_bf16 v[24:27], a[32:35], v[216:219], v[24:27]\n"
    "s_mov_b32 s75, s78\n"
    "v_rcp_f32 v61, v61\n"
    "v_mfma_f32_16x16x32_bf16 v[32:35], a[104:107], v[216:219], v[32:35]\n"
    "s_mov_b32 s77, s80\n"
    "v_fmaak_f32 v62, v18, v62, 0xc0135761\n"
    "v_mul_f32 v62, v62, v38\n"
    "v_mfma_f32_16x16x32_bf16 v[24:27], a[36:39], v[220:223], v[24:27]\n"
    "s_mul_i32 s76, s71, s94\n"
    "v_exp_f32 v62, v62\n"
    "v_mfma_f32_16x16x32_bf16 v[32:35], a[108:111], v[220:223], v[32:35]\n"
    "s_add_i32 s76, s76, s60\n"
    "v_mul_f32 v74, v38, v42\n"
    "v_add_f32 v62, 1.0, v62\n"
    "v_mfma_f32_16x16x32_bf16 v[24:27], a[40:43], v[224:227], v[24:27]\n"
    "s_add_i32 s72, s72, 2\n"
    "v_rcp_f32 v62, v62\n"
    "v_mfma_f32_16x16x32_bf16 v[32:35], a[112:115], v[224:227], v[32:35]\n"
    "v_mul_f32 v63, v39, v39\n"
    "v_fmaak_f32 v63, v18, v63, 0xc0135761\n"
    "v_mfma_f32_16x16x32_bf16 v[24:27], a[44:47], v[228:231], v[24:27]\n"
    "v_mul_f32 v63, v63, v39\n"
    "v_mul_f32 v75, v39, v43\n"
    "v_mfma_f32_16x16x32_bf16 v[32:35], a[116:119], v[228:231], v[32:35]\n"
    "v_exp_f32 v63, v63\n"
    "v_mfma_f32_16x16x32_bf16 v[24:27], a[48:51], v[232:235], v[24:27]\n"
    "global_load_dword v16, v3, s[52:53] sc1\n"
    "v_add_f32 v63, 1.0, v63\n"
    "v_mfma_f32_16x16x32_bf16 v[32:35], a[120:123], v[232:235], v[32:35]\n"
    "v_rcp_f32 v63, v63\n"
    "v_mfma_f32_16x16x32_bf16 v[24:27], a[52:55], v[236:239], v[24:27]\n"
    "v_mul_f32 v72, v72, v60\n"
    "v_mul_f32 v73, v73, v61\n"
    "v_mfma_f32_16x16x32_bf16 v[32:35], a[124:127], v[236:239], v[32:35]\n"
    "v_mul_f32 v74, v74, v62\n"
    "v_mul_f32 v75, v75, v63\n"
    "v_mfma_f32_16x16x32_bf16 v[24:27], a[56:59], v[240:243], v[24:27]\n"
    "v_cvt_pk_bf16_f32 v48, v72, v73\n"
    "v_cvt_pk_bf16_f32 v50, v72, v73\n"
    "v_mfma_f32_16x16x32_bf16 v[32:35], a[128:131], v[240:243], v[32:35]\n"
    "v_cvt_pk_bf16_f32 v49, v74, v75\n"
    "v_cvt_pk_bf16_f32 v51, v74, v75\n"
    "v_mfma_f32_16x16x32_bf16 v[24:27], a[60:63], v[244:247], v[24:27]\n"
    "v_permlane16_swap_b32 v48, v50\n"
    "v_mfma_f32_16x16x32_bf16 v[32:35], a[132:135], v[244:247], v[32:35]\n"
    "s_waitcnt vmcnt(6) lgkmcnt(0)\n"
    "s_barrier\n"
    "ds_read_b128 v[112:115], v1 offset:0\n"
    "v_mfma_f32_16x16x32_bf16 v[24:27], a[64:67], v[248:251], v[24:27]\n"
    "ds_read_b128 v[116:119], v2 offset:0\n"
    "ds_read_b128 v[120:123], v1 offset:1024\n"
    "v_mfma_f32_16x16x32_bf16 v[32:35], a[136:139], v[248:251], v[32:35]\n"
    "ds_read_b128 v[124:127], v2 offset:1024\n"
    "v_permlane16_swap_b32 v49, v51\n"
    "v_mfma_f32_16x16x32_bf16 v[24:27], a[68:71], v[252:255], v[24:27]\n"
    "v_mfma_f32_16x16x32_bf16 v[32:35], a[140:143], v[252:255], v[32:35]\n"
    "s_cmp_eq_u32 s42, 0\n"
    "s_cbranch_scc0 L_body0%=\n"
    "L_idle0%=:\n"
    "s_nop 4\n"
    "v_mov_b32 v19, s83\n"
    "buffer_store_dwordx4 v[48:51], v5, s[44:47], s81 offen sc1\n"
    "v_mul_f32 v56, v24, v24\n"
    "v_fmaak_f32 v56, v18, v56, 0xc0135761\n"
    "v_mul_f32 v56, v56, v24\n"
    "v_exp_f32 v56, v56\n"
    "v_mul_f32 v68, v24, v32\n"
    "v_add_f32 v56, 1.0, v56\n"
    "v_rcp_f32 v56, v56\n"
    "v_mul_f32 v57, v25, v25\n"
    "v_fmaak_f32 v57, v18, v57, 0xc0135761\n"
    "v_mul_f32 v57, v57, v25\n"
    "v_exp_f32 v57, v57\n"
    "v_mul_f32 v69, v25, v33\n"
    "v_add_f32 v57, 1.0, v57\n"
    "v_rcp_f32 v57, v57\n"
    "v_mul_f32 v58, v26, v26\n"
    "v_fmaak_f32 v58, v18, v58, 0xc0135761\n"
    "v_mul_f32 v58, v58, v26\n"
    "v_exp_f32 v58, v58\n"
    "v_mul_f32 v70, v26, v34\n"
    "v_add_f32 v58, 1.0, v58\n"
    "v_rcp_f32 v58, v58\n"
    "v_mul_f32 v59, v27, v27\n"
    "v_fmaak_f32 v59, v18, v59, 0xc0135761\n"
    "v_mul_f32 v59, v59, v27\n"
    "v_exp_f32 v59, v59\n"
    "v_mul_f32 v71, v27, v35\n"
    "v_add_f32 v59, 1.0, v59\n"
    "v_rcp_f32 v59, v59\n"
    "v_mul_f32 v68, v68, v56\n"
    "v_mul_f32 v69, v69, v57\n"
    "v_mul_f32 v70, v70, v58\n"
    "v_mul_f32 v71, v71, v59\n"
    "v_cvt_pk_bf16_f32 v46, v68, v69\n"
    "v_cvt_pk_bf16_f32 v47, v70, v71\n"
    "s_nop 0\n"
    "v_permlane16_swap_b32 v44, v46\n"
    "s_nop 0\n"
    "v_permlane16_swap_b32 v45, v47\n"
    "buffer_store_dwordx4 v[44:47], v4, s[44:47], s81 offen sc1\n"
    "s_waitcnt vmcnt(2)\n"
    "v_add_u32 v16, v16, v11\n"
    "v_cmp_gt_i32 s[92:93], s68, v16\n"
    "v_cmp_gt_i32 vcc, s69, v16\n"
    "s_cmp_eq_u32 vcc_hi, 0\n"
    "s_cselect_b32 s93, 1, 2\n"
    "s_cmp_eq_u32 s92, 0\n"
    "s_cselect_b32 s87, 0, s93\n"
    "v_mov_b32 v17, s87\n"
    "ds_write_b32 v12, v17 offset:16\n"
    "s_waitcnt lgkmcnt(0)\n"
    "s_barrier\n"
    "ds_read_b32 v16, v10 offset:16\n"
    "s_waitcnt lgkmcnt(0)\n"
    "v_readfirstlane_b32 s71, v16\n"
    "s_cmp_lg_u32 s71, 2\n"
    "s_cbranch_scc1 L_go0%=\n"
    "s_waitcnt vmcnt(0)\n"
    "s_barrier\n"
    "s_mov_b64 exec, s[90:91]\n"
    "buffer_store_dword v19, off, s[44:47], s82 sc1\n"
    "s_mov_b64 exec, -1\n"
    "s_mov_b32 s46, 0\n"
    "s_cmp_ge_u32 s72, 128\n"
    "s_cbranch_scc1 L_exit%=\n"
    "L_start%=:\n"
    "s_cmp_lg_u32 s65, 0\n"
    "s_cbranch_scc1 L_got0%=\n"
    "L_poll0%=:\n"
    "global_load_dword v16, v3, s[52:53] sc1\n"
    "s_waitcnt vmcnt(0)\n"
    "v_add_u32 v16, v16, v11\n"
    "v_cmp_gt_i32 s[92:93], s68, v16\n"
    "v_cmp_gt_i32 vcc, s69, v16\n"
    "s_cmp_eq_u32 vcc_hi, 0\n"
    "s_cselect_b32 s93, 1, 2\n"
    "s_cmp_eq_u32 s92, 0\n"
    "s_cselect_b32 s87, 0, s93\n"
    "s_cmp_lg_u32 s87, 2\n"
    "s_cbranch_scc1 L_rdy0%=\n"
    "s_memrealtime s[88:89]\n"
    "s_waitcnt lgkmcnt(0)\n"
    "s_sub_i32 s74, s64, s88\n"
    "s_cmp_ge_u32 s68, 64\n"
    "s_cselect_b32 s87, 1, 0\n"
    "s_cmp_lt_i32 s74, 0\n"
    "s_cbranch_scc1 L_rdy0%=\n"
    "s_sleep 2\n"
    "s_branch L_poll0%=\n"
    "L_rdy0%=:\n"
    "v_mov_b32 v17, s87\n"
    "ds_write_b32 v10, v17 offset:32\n"
    "s_waitcnt lgkmcnt(0)\n"
    "L_got0%=:\n"
    "s_barrier\n"
    "ds_read_b32 v16, v10 offset:32\n"
    "s_waitcnt lgkmcnt(0)\n"
    "v_readfirstlane_b32 s71, v16\n"
    "L_go0%=:\n"
    "s_cmp_eq_u32 s71, 1\n"
    "s_cselect_b32 s80, s69, s68\n"
    "s_cselect_b32 s86, s55, s54\n"
    "s_cselect_b32 s75, s57, s56\n"
    "s_mul_i32 s87, s80, s58\n"
    "s_add_i32 s86, s86, s87\n"
    "s_add_i32 m0, s66, 0\n"
    "s_nop 0\n"
    "buffer_load_dwordx4 v0, s[36:39], s86 offen offset:0 sc1 lds\n"
    "s_add_i32 m0, s66, 896\n"
    "s_nop 0\n"
    "buffer_load_dwordx4 v0, s[36:39], s86 offen offset:128 sc1 lds\n"
    "s_add_i32 m0, s66, 1792\n"
    "s_nop 0\n"
    "buffer_load_dwordx4 v0, s[36:39], s86 offen offset:256 sc1 lds\n"
    "s_add_i32 m0, s66, 2688\n"
    "s_nop 0\n"
    "buffer_load_dwordx4 v0, s[36:39], s86 offen offset:384 sc1 lds\n"
    "s_add_i32 m0, s66, 3584\n"
    "s_nop 0\n"
    "buffer_load_dwordx4 v0, s[36:39], s86 offen offset:512 sc1 lds\n"
    "s_add_i32 m0, s66, 18432\n"
    "s_nop 0\n"
    "buffer_load_dwordx4 v6, s[36:39], s86 offen offset:0 sc1 lds\n"
    "s_add_i32 m0, s66, 19328\n"
    "s_nop 0\n"
    "buffer_load_dwordx4 v6, s[36:39], s86 offen offset:128 sc1 lds\n"
    "s_add_i32 m0, s66, 20224\n"
    "s_nop 0\n"
    "buffer_load_dwordx4 v6, s[36:39], s86 offen offset:256 sc1 lds\n"
    "s_add_i32 m0, s66, 21120\n"
    "s_nop 0\n"
    "buffer_load_dwordx4 v6, s[36:39], s86 offen offset:384 sc1 lds\n"
    "s_add_i32 m0, s66, 22016\n"
    "s_nop 0\n"
    "buffer_load_dwordx4 v6, s[36:39], s86 offen offset:512 sc1 lds\n"
    "s_and_b32 s87, s80, 7\n"
    "s_mul_i32 s87, s87, s59\n"
    "s_add_i32 s75, s75, s87\n"
    "s_and_b32 s92, s71, 1\n"
    "s_add_i32 s69, s69, s92\n"
    "s_lshr_b32 s92, 1, s71\n"
    "s_add_i32 s68, s68, s92\n"
    "s_mov_b32 s77, s80\n"
    "s_mul_i32 s76, s71, s94\n"
    "s_add_i32 s76, s76, s60\n"
    "s_waitcnt vmcnt(5)\n"
    "s_barrier\n"
    "ds_read_b128 v[112:115], v1 offset:0\n"
    "ds_read_b128 v[116:119], v2 offset:0\n"
    "ds_read_b128 v[120:123], v1 offset:1024\n"
    "ds_read_b128 v[124:127], v2 offset:1024\n"
    "global_load_dword v16, v3, s[52:53] sc1\n"
    "s_mov_b64 exec, s[90:91]\n"
    "buffer_store_dword v19, off, s[44:47], s82 sc1\n"
    "s_mov_b64 exec, -1\n"
    "s_mov_b32 s46, 0\n"
    "s_branch L_body0%=\n"
    "L_idle1%=:\n"
    "s_nop 4\n"
    "v_mov_b32 v19, s83\n"
    "buffer_store_dwordx4 v[48:51], v5, s[44:47], s81 offen sc1\n"
    "v_mul_f32 v56, v24, v24\n"
    "v_fmaak_f32 v56, v18, v56, 0xc0135761\n"
    "v_mul_f32 v56, v56, v24\n"
    "v_exp_f32 v56, v56\n"
    "v_mul_f32 v68, v24, v32\n"
    "v_add_f32 v56, 1.0, v56\n"
    "v_rcp_f32 v56, v56\n"
    "v_mul_f32 v57, v25, v25\n"
    "v_fmaak_f32 v57, v18, v57, 0xc0135761\n"
    "v_mul_f32 v57, v57, v25\n"
    "v_exp_f32 v57, v57\n"
    "v_mul_f32 v69, v25, v33\n"
    "v_add_f32 v57, 1.0, v57\n"
    "v_rcp_f32 v57, v57\n"
    "v_mul_f32 v58, v26, v26\n"
    "v_fmaak_f32 v58, v18, v58, 0xc0135761\n"
    "v_mul_f32 v58, v58, v26\n"
    "v_exp_f32 v58, v58\n"
    "v_mul_f32 v70, v26, v34\n"
    "v_add_f32 v58, 1.0, v58\n"
    "v_rcp_f32 v58, v58\n"
    "v_mul_f32 v59, v27, v27\n"
    "v_fmaak_f32 v59, v18, v59, 0xc0135761\n"
    "v_mul_f32 v59, v59, v27\n"
    "v_exp_f32 v59, v59\n"
    "v_mul_f32 v71, v27, v35\n"
    "v_add_f32 v59, 1.0, v59\n"
    "v_rcp_f32 v59, v59\n"
    "v_mul_f32 v68, v68, v56\n"
    "v_mul_f32 v69, v69, v57\n"
    "v_mul_f32 v70, v70, v58\n"
    "v_mul_f32 v71, v71, v59\n"
    "v_cvt_pk_bf16_f32 v46, v68, v69\n"
    "v_cvt_pk_bf16_f32 v47, v70, v71\n"
    "s_nop 0\n"
    "v_permlane16_swap_b32 v44, v46\n"
    "s_nop 0\n"
    "v_permlane16_swap_b32 v45, v47\n"
    "buffer_store_dwordx4 v[44:47], v4, s[44:47], s81 offen sc1\n"
    "s_waitcnt vmcnt(2)\n"
    "v_add_u32 v16, v16, v11\n"
    "v_cmp_gt_i32 s[92:93], s68, v16\n"
    "v_cmp_gt_i32 vcc, s69, v16\n"
    "s_cmp_eq_u32 vcc_hi, 0\n"
    "s_cselect_b32 s93, 1, 2\n"
    "s_cmp_eq_u32 s92, 0\n"
    "s_cselect_b32 s87, 0, s93\n"
    "v_mov_b32 v17, s87\n"
    "ds_write_b32 v12, v17 offset:16\n"
    "s_waitcnt lgkmcnt(0)\n"
    "s_barrier\n"
    "ds_read_b32 v16, v10 offset:16\n"
    "s_waitcnt lgkmcnt(0)\n"
    "v_readfirstlane_b32 s71, v16\n"
    "s_cmp_lg_u32 s71, 2\n"
    "s_cbranch_scc1 L_go1%=\n"
    "s_waitcnt vmcnt(0)\n"
    "s_barrier\n"
    "s_mov_b64 exec, s[90:91]\n"
    "buffer_store_dword v19, off, s[44:47], s82 sc1\n"
    "s_mov_b64 exec, -1\n"
    "s_mov_b32 s46, 0\n"
    "s_cmp_lg_u32 s65, 0\n"
    "s_cbranch_scc1 L_got1%=\n"
    "L_poll1%=:\n"
    "global_load_dword v16, v3, s[52:53] sc1\n"
    "s_waitcnt vmcnt(0)\n"
    "v_add_u32 v16, v16, v11\n"
    "v_cmp_gt_i32 s[92:93], s68, v16\n"
    "v_cmp_gt_i32 vcc, s69, v16\n"
    "s_cmp_eq_u32 vcc_hi, 0\n"
    "s_cselect_b32 s93, 1, 2\n"
    "s_cmp_eq_u32 s92, 0\n"
    "s_cselect_b32 s87, 0, s93\n"
    "s_cmp_lg_u32 s87, 2\n"
    "s_cbranch_scc1 L_rdy1%=\n"
    "s_memrealtime s[88:89]\n"
    "s_waitcnt lgkmcnt(0)\n"
    "s_sub_i32 s74, s64, s88\n"
    "s_cmp_ge_u32 s68, 64\n"
    "s_cselect_b32 s87, 1, 0\n"
    "s_cmp_lt_i32 s74, 0\n"
    "s_cbranch_scc1 L_rdy1%=\n"
    "s_sleep 2\n"
    "s_branch L_poll1%=\n"
    "L_rdy1%=:\n"
    "v_mov_b32 v17, s87\n"
    "ds_write_b32 v10, v17 offset:32\n"
    "s_waitcnt lgkmcnt(0)\n"
    "L_got1%=:\n"
    "s_barrier\n"
    "ds_read_b32 v16, v10 offset:32\n"
    "s_waitcnt lgkmcnt(0)\n"
    "v_readfirstlane_b32 s71, v16\n"
    "L_go1%=:\n"
    "s_cmp_eq_u32 s71, 1\n"
    "s_cselect_b32 s80, s69, s68\n"
    "s_cselect_b32 s86, s55, s54\n"
    "s_cselect_b32 s75, s57, s56\n"
    "s_mul_i32 s87, s80, s58\n"
    "s_add_i32 s86, s86, s87\n"
    "s_add_i32 m0, s66, 36864\n"
    "s_nop 0\n"
    "buffer_load_dwordx4 v0, s[36:39], s86 offen offset:0 sc1 lds\n"
    "s_add_i32 m0, s66, 37760\n"
    "s_nop 0\n"
    "buffer_load_dwordx4 v0, s[36:39], s86 offen offset:128 sc1 lds\n"
    "s_add_i32 m0, s66, 38656\n"
    "s_nop 0\n"
    "buffer_load_dwordx4 v0, s[36:39], s86 offen offset:256 sc1 lds\n"
    "s_add_i32 m0, s66, 39552\n"
    "s_nop 0\n"
    "buffer_load_dwordx4 v0, s[36:39], s86 offen offset:384 sc1 lds\n"
    "s_add_i32 m0, s66, 40448\n"
    "s_nop 0\n"
    "buffer_load_dwordx4 v0, s[36:39], s86 offen offset:512 sc1 lds\n"
    "s_add_i32 m0, s66, 55296\n"
    "s_nop 0\n"
    "buffer_load_dwordx4 v6, s[36:39], s86 offen offset:0 sc1 lds\n"
    "s_add_i32 m0, s66, 56192\n"
    "s_nop 0\n"
    "buffer_load_dwordx4 v6, s[36:39], s86 offen offset:128 sc1 lds\n"
    "s_add_i32 m0, s66, 57088\n"
    "s_nop 0\n"
    "buffer_load_dwordx4 v6, s[36:39], s86 offen offset:256 sc1 lds\n"
    "s_add_i32 m0, s66, 57984\n"
    "s_nop 0\n"
    "buffer_load_dwordx4 v6, s[36:39], s86 offen offset:384 sc1 lds\n"
    "s_add_i32 m0, s66, 58880\n"
    "s_nop 0\n"
    "buffer_load_dwordx4 v6, s[36:39], s86 offen offset:512 sc1 lds\n"
    "s_and_b32 s87, s80, 7\n"
    "s_mul_i32 s87, s87, s59\n"
    "s_add_i32 s75, s75, s87\n"
    "s_and_b32 s92, s71, 1\n"
    "s_add_i32 s69, s69, s92\n"
    "s_lshr_b32 s92, 1, s71\n"
    "s_add_i32 s68, s68, s92\n"
    "s_mov_b32 s77, s80\n"
    "s_mul_i32 s76, s71, s94\n"
    "s_add_i32 s76, s76, s60\n"
    "s_waitcnt vmcnt(5)\n"
    "s_barrier\n"
    "ds_read_b128 v[112:115], v8 offset:0\n"
    "ds_read_b128 v[116:119], v9 offset:0\n"
    "ds_read_b128 v[120:123], v8 offset:1024\n"
    "ds_read_b128 v[124:127], v9 offset:1024\n"
    "global_load_dword v16, v3, s[52:53] sc1\n"
    "s_mov_b64 exec, s[90:91]\n"
    "buffer_store_dword v19, off, s[44:47], s82 sc1\n"
    "s_mov_b64 exec, -1\n"
    "s_mov_b32 s46, 0\n"
    "s_branch L_body1%=\n"
    "L_exit%=:\n"
    : GEMM_OUT : GEMM_IN, GEMM_IN2 : GEMM_CLOBBERS);
  if constexpr (MODE == M_WO) asm volatile(
    "s_nop 4\n"
    "s_mov_b32 s40, s36\n"
    "s_mov_b32 s41, s37\n"
    "s_mov_b32 s42, 0\n"
    "s_mov_b32 s43, s39\n"
    "s_mov_b32 s44, s36\n"
    "s_mov_b32 s45, s37\n"
    "s_mov_b32 s46, 0\n"
    "s_mov_b32 s47, s39\n"
    "s_mov_b32 s68, 0\n"
    "s_mov_b32 s69, 0\n"
    "s_mov_b32 s70, 0\n"
    "s_mov_b32 s72, 0\n"
    "s_mov_b32 s81, 0\n"
    "s_mov_b32 s82, 0\n"
    "s_mov_b32 s83, 0\n"
    "s_mov_b32 s91, 0\n"
    "s_lshr_b32 s90, 1, s65\n"
    "s_lshl_b32 s73, s65, 2\n"
    "v_add_u32 v12, s73, v10\n"
    "v_mov_b32 v18, 0xbdd2d3e8\n"
    "s_branch L_start%=\n"
    "L_body0%=:\n"
    "s_waitcnt lgkmcnt(0)\n"
    "v_mfma_f32_16x16x32_bf16 v[36:39], a[0:3], v[20:23], 0\n"
    "ds_read_b128 v[32:35], v1 offset:3072\n"
    "v_mov_b32 v19, s83\n"
    "v_mfma_f32_16x16x32_bf16 v[36:39], a[4:7], v[24:27], v[36:39]\n"
    "ds_read_b128 v[28:31], v1 offset:2048\n"
    "v_mfma_f32_16x16x32_bf16 v[40:43], a[128:131], v[20:23], 0\n"
    "v_cvt_pk_bf16_f32 v60, v48, v49\n"
    "v_cvt_pk_bf16_f32 v61, v50, v51\n"
    "v_mfma_f32_16x16x32_bf16 v[40:43], a[132:135], v[24:27], v[40:43]\n"
    "v_cvt_pk_bf16_f32 v62, v52, v53\n"
    "v_cvt_pk_bf16_f32 v63, v54, v55\n"
    "v_mfma_f32_16x16x32_bf16 v[44:47], v[80:83], v[20:23], 0\n"
    "v_cvt_pk_bf16_f32 v64, v56, v57\n"
    "v_cvt_pk_bf16_f32 v65, v58, v59\n"
    "v_mfma_f32_16x16x32_bf16 v[44:47], v[84:87], v[24:27], v[44:47]\n"
    "s_waitcnt lgkmcnt(0)\n"
    "ds_write_b64 v6, v[60:61] offset:0\n"
    "ds_write_b64 v6, v[62:63] offset:32\n"
    "v_mfma_f32_16x16x32_bf16 v[36:39], a[8:11], v[28:31], v[36:39]\n"
    "ds_read_b128 v[24:27], v1 offset:5120\n"
    "ds_write_b64 v6, v[64:65] offset:64\n"
    "v_mfma_f32_16x16x32_bf16 v[36:39], a[12:15], v[32:35], v[36:39]\n"
    "ds_read_b128 v[20:23], v1 offset:4096\n"
    "ds_read_b128 v[60:63], v7 offset:0\n"
    "v_mfma_f32_16x16x32_bf16 v[40:43], a[136:139], v[28:31], v[40:43]\n"
    "ds_read_b128 v[64:67], v7 offset:896\n"
    "v_mfma_f32_16x16x32_bf16 v[40:43], a[140:143], v[32:35], v[40:43]\n"
    "v_mfma_f32_16x16x32_bf16 v[44:47], v[88:91], v[28:31], v[44:47]\n"
    "v_mfma_f32_16x16x32_bf16 v[44:47], v[92:95], v[32:35], v[44:47]\n"
    "s_waitcnt lgkmcnt(2)\n"
    "v_mfma_f32_16x16x32_bf16 v[36:39], a[16:19], v[20:23], v[36:39]\n"
    "s_waitcnt lgkmcnt(1)\n"
    "ds_read_b128 v[32:35], v1 offset:7168\n"
    "buffer_store_dwordx4 v[60:63], v4, s[44:47], s81 offen sc1\n"
    "v_mfma_f32_16x16x32_bf16 v[36:39], a[20:23], v[24:27], v[36:39]\n"
    "s_add_i32 s73, s81, s62\n"
    "ds_read_b128 v[28:31], v1 offset:6144\n"
    "v_mfma_f32_16x16x32_bf16 v[40:43], a[144:147], v[20:23], v[40:43]\n"
    "v_mfma_f32_16x16x32_bf16 v[40:43], a[148:151], v[24:27], v[40:43]\n"
    "v_mfma_f32_16x16x32_bf16 v[44:47], v[96:99], v[20:23], v[44:47]\n"
    "v_mfma_f32_16x16x32_bf16 v[44:47], v[100:103], v[24:27], v[44:47]\n"
    "s_waitcnt lgkmcnt(0)\n"
    "s_waitcnt lgkmcnt(2)\n"
    "buffer_store_dwordx4 v[64:67], v4, s[44:47], s73 offen sc1\n"
    "v_mfma_f32_16x16x32_bf16 v[36:39], a[24:27], v[28:31], v[36:39]\n"
    "ds_read_b128 v[24:27], v1 offset:9216\n"
    "v_mfma_f32_16x16x32_bf16 v[36:39], a[28:31], v[32:35], v[36:39]\n"
    "ds_read_b128 v[20:23], v1 offset:8192\n"
    "v_mfma_f32_16x16x32_bf16 v[40:43], a[152:155], v[28:31], v[40:43]\n"
    "s_waitcnt vmcnt(2)\n"
    "v_add_u32 v16, v16, v11\n"
    "v_mfma_f32_16x16x32_bf16 v[40:43], a[156:159], v[32:35], v[40:43]\n"
    "v_cmp_gt_i32 s[92:93], s68, v16\n"
    "v_mfma_f32_16x16x32_bf16 v[44:47], v[104:107], v[28:31], v[44:47]\n"
    "v_cmp_gt_i32 vcc, s69, v16\n"
    "v_mfma_f32_16x16x32_bf16 v[44:47], v[108:111], v[32:35], v[44:47]\n"
    "s_waitcnt lgkmcnt(0)\n"
    "v_mfma_f32_16x16x32_bf16 v[36:39], a[32:35], v[20:23], v[36:39]\n"
    "s_cmp_eq_u32 vcc_hi, 0\n"
    "s_cselect_b32 s93, 1, 2\n"
    "ds_read_b128 v[32:35], v1 offset:11264\n"
    "v_mfma_f32_16x16x32_bf16 v[36:39], a[36:39], v[24:27], v[36:39]\n"
    "s_cmp_eq_u32 s92, 0\n"
    "s_cselect_b32 s87, 0, s93\n"
    "ds_read_b128 v[28:31], v1 offset:10240\n"
    "v_mfma_f32_16x16x32_bf16 v[40:43], a[160:163], v[20:23], v[40:43]\n"
    "v_mov_b32 v17, s87\n"
    "v_mfma_f32_16x16x32_bf16 v[40:43], a[164:167], v[24:27], v[40:43]\n"
    "ds_write_b32 v12, v17\n"
    "v_mfma_f32_16x16x32_bf16 v[44:47], v[112:115], v[20:23], v[44:47]\n"
    "v_mfma_f32_16x16x32_bf16 v[44:47], v[116:119], v[24:27], v[44:47]\n"
    "s_waitcnt lgkmcnt(1)\n"
    "v_mfma_f32_16x16x32_bf16 v[36:39], a[40:43], v[28:31], v[36:39]\n"
    "s_waitcnt lgkmcnt(0)\n"
    "s_barrier\n"
    "ds_read_b32 v16, v10\n"
    "v_mfma_f32_16x16x32_bf16 v[36:39], a[44:47], v[32:35], v[36:39]\n"
    "ds_read_b128 v[24:27], v1 offset:13312\n"
    "ds_read_b128 v[20:23], v1 offset:12288\n"
    "v_mfma_f32_16x16x32_bf16 v[40:43], a[168:171], v[28:31], v[40:43]\n"
    "v_mfma_f32_16x16x32_bf16 v[40:43], a[172:175], v[32:35], v[40:43]\n"
    "v_mfma_f32_16x16x32_bf16 v[44:47], v[120:123], v[28:31], v[44:47]\n"
    "s_waitcnt lgkmcnt(2)\n"
    "v_readfirstlane_b32 s71, v16\n"
    "v_mfma_f32_16x16x32_bf16 v[44:47], v[124:127], v[32:35], v[44:47]\n"
    "s_waitcnt lgkmcnt(0)\n"
    "s_cmp_lg_u32 s71, 2\n"
    "s_cselect_b32 s42, s38, 0\n"
    "v_mfma_f32_16x16x32_bf16 v[36:39], a[48:51], v[20:23], v[36:39]\n"
    "s_cmp_eq_u32 s71, 1\n"
    "s_cselect_b32 s80, s69, s68\n"
    "ds_read_b128 v[32:35], v1 offset:15360\n"
    "v_mfma_f32_16x16x32_bf16 v[36:39], a[52:55], v[24:27], v[36:39]\n"
    "s_cselect_b32 s86, s55, s54\n"
    "s_cselect_b32 s78, s57, s56\n"
    "ds_read_b128 v[28:31], v1 offset:14336\n"
    "v_mfma_f32_16x16x32_bf16 v[40:43], a[176:179], v[20:23], v[40:43]\n"
    "s_and_b32 s87, s80, 7\n"
    "s_mul_i32 s87, s87, s58\n"
    "s_add_i32 s86, s86, s87\n"
    "v_mfma_f32_16x16x32_bf16 v[40:43], a[180:183], v[24:27], v[40:43]\n"
    "v_mfma_f32_16x16x32_bf16 v[44:47], v[128:131], v[20:23], v[44:47]\n"
    "v_mfma_f32_16x16x32_bf16 v[44:47], v[132:135], v[24:27], v[44:47]\n"
    "s_waitcnt lgkmcnt(0)\n"
    "s_add_i32 m0, s66, 32768\n"
    "s_add_i32 s74, s86, 0\n"
    "v_mfma_f32_16x16x32_bf16 v[36:39], a[56:59], v[28:31], v[36:39]\n"
    "ds_read_b128 v[24:27], v1 offset:17408\n"
    "buffer_load_dwordx4 v0, s[40:43], s74 offen offset:0 sc1 lds\n"
    "v_mfma_f32_16x16x32_bf16 v[36:39], a[60:63], v[32:35], v[36:39]\n"
    "ds_read_b128 v[20:23], v1 offset:16384\n"
    "v_mfma_f32_16x16x32_bf16 v[40:43], a[184:187], v[28:31], v[40:43]\n"
    "s_and_b32 s87, s80, 7\n"
    "s_mul_i32 s87, s87, s59\n"
    "s_add_i32 s78, s78, s87\n"
    "v_mfma_f32_16x16x32_bf16 v[40:43], a[188:191], v[32:35], v[40:43]\n"
    "s_and_b32 s92, s71, 1\n"
    "s_add_i32 s69, s69, s92\n"
    "s_lshr_b32 s92, 1, s71\n"
    "v_mfma_f32_16x16x32_bf16 v[44:47], v[136:139], v[28:31], v[44:47]\n"
    "s_add_i32 s68, s68, s92\n"
    "buffer_load_dwordx4 v0, s[40:43], s74 offen offset:1024 sc1 lds\n"
    "v_mfma_f32_16x16x32_bf16 v[44:47], v[140:143], v[32:35], v[44:47]\n"
    "s_waitcnt lgkmcnt(0)\n"
    "v_mfma_f32_16x16x32_bf16 v[36:39], a[64:67], v[20:23], v[36:39]\n"
    "ds_read_b128 v[32:35], v1 offset:19456\n"
    "v_mfma_f32_16x16x32_bf16 v[36:39], a[68:71], v[24:27], v[36:39]\n"
    "ds_read_b128 v[28:31], v1 offset:18432\n"
    "v_mfma_f32_16x16x32_bf16 v[40:43], a[192:195], v[20:23], v[40:43]\n"
    "buffer_load_dwordx4 v0, s[40:43], s74 offen offset:2048 sc1 lds\n"
    "v_mfma_f32_16x16x32_bf16 v[40:43], a[196:199], v[24:27], v[40:43]\n"
    "v_mfma_f32_16x16x32_bf16 v[44:47], v[144:147], v[20:23], v[44:47]\n"
    "v_mfma_f32_16x16x32_bf16 v[44:47], v[148:151], v[24:27], v[44:47]\n"
    "s_waitcnt lgkmcnt(0)\n"
    "v_mfma_f32_16x16x32_bf16 v[36:39], a[72:75], v[28:31], v[36:39]\n"
    "ds_read_b128 v[24:27], v1 offset:21504\n"
    "buffer_load_dwordx4 v0, s[40:43], s74 offen offset:3072 sc1 lds\n"
    "v_mfma_f32_16x16x32_bf16 v[36:39], a[76:79], v[32:35], v[36:39]\n"
    "ds_read_b128 v[20:23], v1 offset:20480\n"
    "v_mfma_f32_16x16x32_bf16 v[40:43], a[200:203], v[28:31], v[40:43]\n"
    "v_mfma_f32_16x16x32_bf16 v[40:43], a[204:207], v[32:35], v[40:43]\n"
    "v_mfma_f32_16x16x32_bf16 v[44:47], v[152:155], v[28:31], v[44:47]\n"
    "v_mfma_f32_16x16x32_bf16 v[44:47], v[156:159], v[32:35], v[44:47]\n"
    "s_waitcnt lgkmcnt(0)\n"
    "s_add_i32 m0, s66, 49152\n"
    "s_add_i32 s74, s86, 32768\n"
    "v_mfma_f32_16x16x32_bf16 v[36:39], a[80:83], v[20:23], v[36:39]\n"
    "ds_read_b128 v[32:35], v1 offset:23552\n"
    "buffer_load_dwordx4 v0, s[40:43], s74 offen offset:0 sc1 lds\n"
    "v_mfma_f32_16x16x32_bf16 v[36:39], a[84:87], v[24:27], v[36:39]\n"
    "s_waitcnt vmcnt(5)\n"
    "s_barrier\n"
    "ds_read_b128 v[28:31], v1 offset:22528\n"
    "v_mfma_f32_16x16x32_bf16 v[40:43], a[208:211], v[20:23], v[40:43]\n"
    "s_mov_b64 exec, s[90:91]\n"
    "buffer_store_dword v19, off, s[44:47], s82 sc1\n"
    "s_mov_b64 exec, -1\n"
    "v_mfma_f32_16x16x32_bf16 v[40:43], a[212:215], v[24:27], v[40:43]\n"
    "s_mov_b32 s46, s38\n"
    "s_mov_b32 s81, s75\n"
    "s_mov_b32 s82, s76\n"
    "v_mfma_f32_16x16x32_bf16 v[44:47], v[160:163], v[20:23], v[44:47]\n"
    "s_mov_b32 s83, s77\n"
    "s_mov_b32 s75, s78\n"
    "s_mov_b32 s77, s80\n"
    "v_mfma_f32_16x16x32_bf16 v[44:47], v[164:167], v[24:27], v[44:47]\n"
    "s_waitcnt lgkmcnt(0)\n"
    "s_mul_i32 s76, s71, s94\n"
    "buffer_load_dwordx4 v0, s[40:43], s74 offen offset:1024 sc1 lds\n"
    "v_mfma_f32_16x16x32_bf16 v[36:39], a[88:91], v[28:31], v[36:39]\n"
    "s_add_i32 s76, s76, s60\n"
    "ds_read_b128 v[24:27], v1 offset:25600\n"
    "v_mfma_f32_16x16x32_bf16 v[36:39], a[92:95], v[32:35], v[36:39]\n"
    "ds_read_b128 v[20:23], v1 offset:24576\n"
    "v_mfma_f32_16x16x32_bf16 v[40:43], a[216:219], v[28:31], v[40:43]\n"
    "v_mfma_f32_16x16x32_bf16 v[40:43], a[220:223], v[32:35], v[40:43]\n"
    "v_mfma_f32_16x16x32_bf16 v[44:47], v[168:171], v[28:31], v[44:47]\n"
    "buffer_load_dwordx4 v0, s[40:43], s74 offen offset:2048 sc1 lds\n"
    "v_mfma_f32_16x16x32_bf16 v[44:47], v[172:175], v[32:35], v[44:47]\n"
    "s_waitcnt lgkmcnt(0)\n"
    "v_mfma_f32_16x16x32_bf16 v[36:39], a[96:99], v[20:23], v[36:39]\n"
    "ds_read_b128 v[32:35], v1 offset:27648\n"
    "v_mfma_f32_16x16x32_bf16 v[36:39], a[100:103], v[24:27], v[36:39]\n"
    "ds_read_b128 v[28:31], v1 offset:26624\n"
    "v_mfma_f32_16x16x32_bf16 v[40:43], a[224:227], v[20:23], v[40:43]\n"
    "v_mfma_f32_16x16x32_bf16 v[40:43], a[228:231], v[24:27], v[40:43]\n"
    "buffer_load_dwordx4 v0, s[40:43], s74 offen offset:3072 sc1 lds\n"
    "v_mfma_f32_16x16x32_bf16 v[44:47], v[176:179], v[20:23], v[44:47]\n"
    "v_mfma_f32_16x16x32_bf16 v[44:47], v[180:183], v[24:27], v[44:47]\n"
    "s_waitcnt lgkmcnt(0)\n"
    "v_mfma_f32_16x16x32_bf16 v[36:39], a[104:107], v[28:31], v[36:39]\n"
    "ds_read_b128 v[24:27], v1 offset:29696\n"
    "v_mfma_f32_16x16x32_bf16 v[36:39], a[108:111], v[32:35], v[36:39]\n"
    "ds_read_b128 v[20:23], v1 offset:28672\n"
    "v_mfma_f32_16x16x32_bf16 v[40:43], a[232:235], v[28:31], v[40:43]\n"
    "v_mfma_f32_16x16x32_bf16 v[40:43], a[236:239], v[32:35], v[40:43]\n"
    "v_mfma_f32_16x16x32_bf16 v[44:47], v[184:187], v[28:31], v[44:47]\n"
    "v_mfma_f32_16x16x32_bf16 v[44:47], v[188:191], v[32:35], v[44:47]\n"
    "s_waitcnt lgkmcnt(0)\n"
    "v_mfma_f32_16x16x32_bf16 v[36:39], a[112:115], v[20:23], v[36:39]\n"
    "ds_read_b128 v[32:35], v1 offset:31744\n"
    "global_load_dword v16, v3, s[52:53] sc1\n"
    "v_mfma_f32_16x16x32_bf16 v[36:39], a[116:119], v[24:27], v[36:39]\n"
    "ds_read_b128 v[28:31], v1 offset:30720\n"
    "v_mfma_f32_16x16x32_bf16 v[40:43], a[240:243], v[20:23], v[40:43]\n"
    "v_mfma_f32_16x16x32_bf16 v[40:43], a[244:247], v[24:27], v[40:43]\n"
    "v_mfma_f32_16x16x32_bf16 v[44:47], v[192:195], v[20:23], v[44:47]\n"
    "v_mfma_f32_16x16x32_bf16 v[44:47], v[196:199], v[24:27], v[44:47]\n"
    "s_waitcnt lgkmcnt(0)\n"
    "v_mfma_f32_16x16x32_bf16 v[36:39], a[120:123], v[28:31], v[36:39]\n"
    "v_mfma_f32_16x16x32_bf16 v[36:39], a[124:127], v[32:35], v[36:39]\n"
    "s_waitcnt vmcnt(5) lgkmcnt(0)\n"
    "s_barrier\n"
    "ds_read_b128 v[24:27], v8 offset:1024\n"
    "v_mfma_f32_16x16x32_bf16 v[40:43], a[248:251], v[28:31], v[40:43]\n"
    "ds_read_b128 v[20:23], v8 offset:0\n"
    "v_mfma_f32_16x16x32_bf16 v[40:43], a[252:255], v[32:35], v[40:43]\n"
    "v_mfma_f32_16x16x32_bf16 v[44:47], v[200:203], v[28:31], v[44:47]\n"
    "v_mfma_f32_16x16x32_bf16 v[44:47], v[204:207], v[32:35], v[44:47]\n"
    "s_cmp_eq_u32 s42, 0\n"
    "s_cbranch_scc1 L_idle1%=\n"
    "L_body1%=:\n"
    "s_waitcnt lgkmcnt(0)\n"
    "v_mfma_f32_16x16x32_bf16 v[48:51], a[0:3], v[20:23], 0\n"
    "ds_read_b128 v[32:35], v8 offset:3072\n"
    "v_mov_b32 v19, s83\n"
    "v_mfma_f32_16x16x32_bf16 v[48:51], a[4:7], v[24:27], v[48:51]\n"
    "ds_read_b128 v[28:31], v8 offset:2048\n"
    "v_mfma_f32_16x16x32_bf16 v[52:55], a[128:131], v[20:23], 0\n"
    "v_cvt_pk_bf16_f32 v60, v36, v37\n"
    "v_cvt_pk_bf16_f32 v61, v38, v39\n"
    "v_mfma_f32_16x16x32_bf16 v[52:55], a[132:135], v[24:27], v[52:55]\n"
    "v_cvt_pk_bf16_f32 v62, v40, v41\n"
    "v_cvt_pk_bf16_f32 v63, v42, v43\n"
    "v_mfma_f32_16x16x32_bf16 v[56:59], v[80:83], v[20:23], 0\n"
    "v_cvt_pk_bf16_f32 v64, v44, v45\n"
    "v_cvt_pk_bf16_f32 v65, v46, v47\n"
    "v_mfma_f32_16x16x32_bf16 v[56:59], v[84:87], v[24:27], v[56:59]\n"
    "s_waitcnt lgkmcnt(0)\n"
    "ds_write_b64 v6, v[60:61] offset:0\n"
    "ds_write_b64 v6, v[62:63] offset:32\n"
    "v_mfma_f32_16x16x32_bf16 v[48:51], a[8:11], v[28:31], v[48:51]\n"
    "ds_read_b128 v[24:27], v8 offset:5120\n"
    "ds_write_b64 v6, v[64:65] offset:64\n"
    "v_mfma_f32_16x16x32_bf16 v[48:51], a[12:15], v[32:35], v[48:51]\n"
    "ds_read_b128 v[20:23], v8 offset:4096\n"
    "ds_read_b128 v[60:63], v7 offset:0\n"
    "v_mfma_f32_16x16x32_bf16 v[52:55], a[136:139], v[28:31], v[52:55]\n"
    "ds_read_b128 v[64:67], v7 offset:896\n"
    "v_mfma_f32_16x16x32_bf16 v[52:55], a[140:143], v[32:35], v[52:55]\n"
    "v_mfma_f32_16x16x32_bf16 v[56:59], v[88:91], v[28:31], v[56:59]\n"
    "v_mfma_f32_16x16x32_bf16 v[56:59], v[92:95], v[32:35], v[56:59]\n"
    "s_waitcnt lgkmcnt(2)\n"
    "v_mfma_f32_16x16x32_bf16 v[48:51], a[16:19], v[20:23], v[48:51]\n"
    "s_waitcnt lgkmcnt(1)\n"
    "ds_read_b128 v[32:35], v8 offset:7168\n"
    "buffer_store_dwordx4 v[60:63], v4, s[44:47], s81 offen sc1\n"
    "v_mfma_f32_16x16x32_bf16 v[48:51], a[20:23], v[24:27], v[48:51]\n"
    "s_add_i32 s73, s81, s62\n"
    "ds_read_b128 v[28:31], v8 offset:6144\n"
    "v_mfma_f32_16x16x32_bf16 v[52:55], a[144:147], v[20:23], v[52:55]\n"
    "v_mfma_f32_16x16x32_bf16 v[52:55], a[148:151], v[24:27], v[52:55]\n"
    "v_mfma_f32_16x16x32_bf16 v[56:59], v[96:99], v[20:23], v[56:59]\n"
    "v_mfma_f32_16x16x32_bf16 v[56:59], v[100:103], v[24:27], v[56:59]\n"
    "s_waitcnt lgkmcnt(0)\n"
    "s_waitcnt lgkmcnt(2)\n"
    "buffer_store_dwordx4 v[64:67], v4, s[44:47], s73 offen sc1\n"
    "v_mfma_f32_16x16x32_bf16 v[48:51], a[24:27], v[28:31], v[48:51]\n"
    "ds_read_b128 v[24:27], v8 offset:9216\n"
    "v_mfma_f32_16x16x32_bf16 v[48:51], a[28:31], v[32:35], v[48:51]\n"
    "ds_read_b128 v[20:23], v8 offset:8192\n"
    "v_mfma_f32_16x16x32_bf16 v[52:55], a[152:155], v[28:31], v[52:55]\n"
    "s_waitcnt vmcnt(2)\n"
    "v_add_u32 v16, v16, v11\n"
    "v_mfma_f32_16x16x32_bf16 v[52:55], a[156:159], v[32:35], v[52:55]\n"
    "v_cmp_gt_i32 s[92:93], s68, v16\n"
    "v_mfma_f32_16x16x32_bf16 v[56:59], v[104:107], v[28:31], v[56:59]\n"
    "v_cmp_gt_i32 vcc, s69, v16\n"
    "v_mfma_f32_16x16x32_bf16 v[56:59], v[108:111], v[32:35], v[56:59]\n"
    "s_waitcnt lgkmcnt(0)\n"
    "v_mfma_f32_16x16x32_bf16 v[48:51], a[32:35], v[20:23], v[48:51]\n"
    "s_cmp_eq_u32 vcc_hi, 0\n"
    "s_cselect_b32 s93, 1, 2\n"
    "ds_read_b128 v[32:35], v8 offset:11264\n"
    "v_mfma_f32_16x16x32_bf16 v[48:51], a[36:39], v[24:27], v[48:51]\n"
    "s_cmp_eq_u32 s92, 0\n"
    "s_cselect_b32 s87, 0, s93\n"
    "ds_read_b128 v[28:31], v8 offset:10240\n"
    "v_mfma_f32_16x16x32_bf16 v[52:55], a[160:163], v[20:23], v[52:55]\n"
    "v_mov_b32 v17, s87\n"
    "v_mfma_f32_16x16x32_bf16 v[52:55], a[164:167], v[24:27], v[52:55]\n"
    "ds_write_b32 v12, v17\n"
    "v_mfma_f32_16x16x32_bf16 v[56:59], v[112:115], v[20:23], v[56:59]\n"
    "v_mfma_f32_16x16x32_bf16 v[56:59], v[116:119], v[24:27], v[56:59]\n"
    "s_waitcnt lgkmcnt(1)\n"
    "v_mfma_f32_16x16x32_bf16 v[48:51], a[40:43], v[28:31], v[48:51]\n"
    "s_waitcnt lgkmcnt(0)\n"
    "s_barrier\n"
    "ds_read_b32 v16, v10\n"
    "v_mfma_f32_16x16x32_bf16 v[48:51], a[44:47], v[32:35], v[48:51]\n"
    "ds_read_b128 v[24:27], v8 offset:13312\n"
    "ds_read_b128 v[20:23], v8 offset:12288\n"
    "v_mfma_f32_16x16x32_bf16 v[52:55], a[168:171], v[28:31], v[52:55]\n"
    "v_mfma_f32_16x16x32_bf16 v[52:55], a[172:175], v[32:35], v[52:55]\n"
    "v_mfma_f32_16x16x32_bf16 v[56:59], v[120:123], v[28:31], v[56:59]\n"
    "s_waitcnt lgkmcnt(2)\n"
    "v_readfirstlane_b32 s71, v16\n"
    "v_mfma_f32_16x16x32_bf16 v[56:59], v[124:127], v[32:35], v[56:59]\n"
    "s_waitcnt lgkmcnt(0)\n"
    "s_cmp_lg_u32 s71, 2\n"
    "s_cselect_b32 s42, s38, 0\n"
    "v_mfma_f32_16x16x32_bf16 v[48:51], a[48:51], v[20:23], v[48:51]\n"
    "s_cmp_eq_u32 s71, 1\n"
    "s_cselect_b32 s80, s69, s68\n"
    "ds_read_b128 v[32:35], v8 offset:15360\n"
    "v_mfma_f32_16x16x32_bf16 v[48:51], a[52:55], v[24:27], v[48:51]\n"
    "s_cselect_b32 s86, s55, s54\n"
    "s_cselect_b32 s78, s57, s56\n"
    "ds_read_b128 v[28:31], v8 offset:14336\n"
    "v_mfma_f32_16x16x32_bf16 v[52:55], a[176:179], v[20:23], v[52:55]\n"
    "s_and_b32 s87, s80, 7\n"
    "s_mul_i32 s87, s87, s58\n"
    "s_add_i32 s86, s86, s87\n"
    "v_mfma_f32_16x16x32_bf16 v[52:55], a[180:183], v[24:27], v[52:55]\n"
    "v_mfma_f32_16x16x32_bf16 v[56:59], v[128:131], v[20:23], v[56:59]\n"
    "v_mfma_f32_16x16x32_bf16 v[56:59], v[132:135], v[24:27], v[56:59]\n"
    "s_waitcnt lgkmcnt(0)\n"
    "s_add_i32 m0, s66, 0\n"
    "s_add_i32 s74, s86, 0\n"
    "v_mfma_f32_16x16x32_bf16 v[48:51], a[56:59], v[28:31], v[48:51]\n"
    "ds_read_b128 v[24:27], v8 offset:17408\n"
    "buffer_load_dwordx4 v0, s[40:43], s74 offen offset:0 sc1 lds\n"
    "v_mfma_f32_16x16x32_bf16 v[48:51], a[60:63], v[32:35], v[48:51]\n"
    "ds_read_b128 v[20:23], v8 offset:16384\n"
    "v_mfma_f32_16x16x32_bf16 v[52:55], a[184:187], v[28:31], v[52:55]\n"
    "s_and_b32 s87, s80, 7\n"
    "s_mul_i32 s87, s87, s59\n"
    "s_add_i32 s78, s78, s87\n"
    "v_mfma_f32_16x16x32_bf16 v[52:55], a[188:191], v[32:35], v[52:55]\n"
    "s_and_b32 s92, s71, 1\n"
    "s_add_i32 s69, s69, s92\n"
    "s_lshr_b32 s92, 1, s71\n"
    "v_mfma_f32_16x16x32_bf16 v[56:59], v[136:139], v[28:31], v[56:59]\n"
    "s_add_i32 s68, s68, s92\n"
    "buffer_load_dwordx4 v0, s[40:43], s74 offen offset:1024 sc1 lds\n"
    "v_mfma_f32_16x16x32_bf16 v[56:59], v[140:143], v[32:35], v[56:59]\n"
    "s_waitcnt lgkmcnt(0)\n"
    "v_mfma_f32_16x16x32_bf16 v[48:51], a[64:67], v[20:23], v[48:51]\n"
    "ds_read_b128 v[32:35], v8 offset:19456\n"
    "v_mfma_f32_16x16x32_bf16 v[48:51], a[68:71], v[24:27], v[48:51]\n"
    "ds_read_b128 v[28:31], v8 offset:18432\n"
    "v_mfma_f32_16x16x32_bf16 v[52:55], a[192:195], v[20:23], v[52:55]\n"
    "buffer_load_dwordx4 v0, s[40:43], s74 offen offset:2048 sc1 lds\n"
    "v_mfma_f32_16x16x32_bf16 v[52:55], a[196:199], v[24:27], v[52:55]\n"
    "v_mfma_f32_16x16x32_bf16 v[56:59], v[144:147], v[20:23], v[56:59]\n"
    "v_mfma_f32_16x16x32_bf16 v[56:59], v[148:151], v[24:27], v[56:59]\n"
    "s_waitcnt lgkmcnt(0)\n"
    "v_mfma_f32_16x16x32_bf16 v[48:51], a[72:75], v[28:31], v[48:51]\n"
    "ds_read_b128 v[24:27], v8 offset:21504\n"
    "buffer_load_dwordx4 v0, s[40:43], s74 offen offset:3072 sc1 lds\n"
    "v_mfma_f32_16x16x32_bf16 v[48:51], a[76:79], v[32:35], v[48:51]\n"
    "ds_read_b128 v[20:23], v8 offset:20480\n"
    "v_mfma_f32_16x16x32_bf16 v[52:55], a[200:203], v[28:31], v[52:55]\n"
    "v_mfma_f32_16x16x32_bf16 v[52:55], a[204:207], v[32:35], v[52:55]\n"
    "v_mfma_f32_16x16x32_bf16 v[56:59], v[152:155], v[28:31], v[56:59]\n"
    "v_mfma_f32_16x16x32_bf16 v[56:59], v[156:159], v[32:35], v[56:59]\n"
    "s_waitcnt lgkmcnt(0)\n"
    "s_add_i32 m0, s66, 16384\n"
    "s_add_i32 s74, s86, 32768\n"
    "v_mfma_f32_16x16x32_bf16 v[48:51], a[80:83], v[20:23], v[48:51]\n"
    "ds_read_b128 v[32:35], v8 offset:23552\n"
    "buffer_load_dwordx4 v0, s[40:43], s74 offen offset:0 sc1 lds\n"
    "v_mfma_f32_16x16x32_bf16 v[48:51], a[84:87], v[24:27], v[48:51]\n"
    "s_waitcnt vmcnt(5)\n"
    "s_barrier\n"
    "ds_read_b128 v[28:31], v8 offset:22528\n"
    "v_mfma_f32_16x16x32_bf16 v[52:55], a[208:211], v[20:23], v[52:55]\n"
    "s_mov_b64 exec, s[90:91]\n"
    "buffer_store_dword v19, off, s[44:47], s82 sc1\n"
    "s_mov_b64 exec, -1\n"
    "v_mfma_f32_16x16x32_bf16 v[52:55], a[212:215], v[24:27], v[52:55]\n"
    "s_mov_b32 s46, s38\n"
    "s_mov_b32 s81, s75\n"
    "s_mov_b32 s82, s76\n"
    "v_mfma_f32_16x16x32_bf16 v[56:59], v[160:163], v[20:23], v[56:59]\n"
    "s_mov_b32 s83, s77\n"
    "s_mov_b32 s75, s78\n"
    "s_mov_b32 s77, s80\n"
    "v_mfma_f32_16x16x32_bf16 v[56:59], v[164:167], v[24:27], v[56:59]\n"
    "s_waitcnt lgkmcnt(0)\n"
    "s_mul_i32 s76, s71, s94\n"
    "buffer_load_dwordx4 v0, s[40:43], s74 offen offset:1024 sc1 lds\n"
    "v_mfma_f32_16x16x32_bf16 v[48:51], a[88:91], v[28:31], v[48:51]\n"
    "s_add_i32 s76, s76, s60\n"
    "s_add_i32 s72, s72, 2\n"
    "ds_read_b128 v[24:27], v8 offset:25600\n"
    "v_mfma_f32_16x16x32_bf16 v[48:51], a[92:95], v[32:35], v[48:51]\n"
    "ds_read_b128 v[20:23], v8 offset:24576\n"
    "v_mfma_f32_16x16x32_bf16 v[52:55], a[216:219], v[28:31], v[52:55]\n"
    "v_mfma_f32_16x16x32_bf16 v[52:55], a[220:223], v[32:35], v[52:55]\n"
    "v_mfma_f32_16x16x32_bf16 v[56:59], v[168:171], v[28:31], v[56:59]\n"
    "buffer_load_dwordx4 v0, s[40:43], s74 offen offset:2048 sc1 lds\n"
    "v_mfma_f32_16x16x32_bf16 v[56:59], v[172:175], v[32:35], v[56:59]\n"
    "s_waitcnt lgkmcnt(0)\n"
    "v_mfma_f32_16x16x32_bf16 v[48:51], a[96:99], v[20:23], v[48:51]\n"
    "ds_read_b128 v[32:35], v8 offset:27648\n"
    "v_mfma_f32_16x16x32_bf16 v[48:51], a[100:103], v[24:27], v[48:51]\n"
    "ds_read_b128 v[28:31], v8 offset:26624\n"
    "v_mfma_f32_16x16x32_bf16 v[52:55], a[224:227], v[20:23], v[52:55]\n"
    "v_mfma_f32_16x16x32_bf16 v[52:55], a[228:231], v[24:27], v[52:55]\n"
    "buffer_load_dwordx4 v0, s[40:43], s74 offen offset:3072 sc1 lds\n"
    "v_mfma_f32_16x16x32_bf16 v[56:59], v[176:179], v[20:23], v[56:59]\n"
    "v_mfma_f32_16x16x32_bf16 v[56:59], v[180:183], v[24:27], v[56:59]\n"
    "s_waitcnt lgkmcnt(0)\n"
    "v_mfma_f32_16x16x32_bf16 v[48:51], a[104:107], v[28:31], v[48:51]\n"
    "ds_read_b128 v[24:27], v8 offset:29696\n"
    "v_mfma_f32_16x16x32_bf16 v[48:51], a[108:111], v[32:35], v[48:51]\n"
    "ds_read_b128 v[20:23], v8 offset:28672\n"
    "v_mfma_f32_16x16x32_bf16 v[52:55], a[232:235], v[28:31], v[52:55]\n"
    "v_mfma_f32_16x16x32_bf16 v[52:55], a[236:239], v[32:35], v[52:55]\n"
    "v_mfma_f32_16x16x32_bf16 v[56:59], v[184:187], v[28:31], v[56:59]\n"
    "v_mfma_f32_16x16x32_bf16 v[56:59], v[188:191], v[32:35], v[56:59]\n"
    "s_waitcnt lgkmcnt(0)\n"
    "v_mfma_f32_16x16x32_bf16 v[48:51], a[112:115], v[20:23], v[48:51]\n"
    "ds_read_b128 v[32:35], v8 offset:31744\n"
    "global_load_dword v16, v3, s[52:53] sc1\n"
    "v_mfma_f32_16x16x32_bf16 v[48:51], a[116:119], v[24:27], v[48:51]\n"
    "ds_read_b128 v[28:31], v8 offset:30720\n"
    "v_mfma_f32_16x16x32_bf16 v[52:55], a[240:243], v[20:23], v[52:55]\n"
    "v_mfma_f32_16x16x32_bf16 v[52:55], a[244:247], v[24:27], v[52:55]\n"
    "v_mfma_f32_16x16x32_bf16 v[56:59], v[192:195], v[20:23], v[56:59]\n"
    "v_mfma_f32_16x16x32_bf16 v[56:59], v[196:199], v[24:27], v[56:59]\n"
    "s_waitcnt lgkmcnt(0)\n"
    "v_mfma_f32_16x16x32_bf16 v[48:51], a[120:123], v[28:31], v[48:51]\n"
    "v_mfma_f32_16x16x32_bf16 v[48:51], a[124:127], v[32:35], v[48:51]\n"
    "s_waitcnt vmcnt(5) lgkmcnt(0)\n"
    "s_barrier\n"
    "ds_read_b128 v[24:27], v1 offset:1024\n"
    "v_mfma_f32_16x16x32_bf16 v[52:55], a[248:251], v[28:31], v[52:55]\n"
    "ds_read_b128 v[20:23], v1 offset:0\n"
    "v_mfma_f32_16x16x32_bf16 v[52:55], a[252:255], v[32:35], v[52:55]\n"
    "v_mfma_f32_16x16x32_bf16 v[56:59], v[200:203], v[28:31], v[56:59]\n"
    "v_mfma_f32_16x16x32_bf16 v[56:59], v[204:207], v[32:35], v[56:59]\n"
    "s_cmp_eq_u32 s42, 0\n"
    "s_cbranch_scc0 L_body0%=\n"
    "L_idle0%=:\n"
    "s_nop 4\n"
    "v_mov_b32 v19, s83\n"
    "v_cvt_pk_bf16_f32 v60, v48, v49\n"
    "v_cvt_pk_bf16_f32 v61, v50, v51\n"
    "v_cvt_pk_bf16_f32 v62, v52, v53\n"
    "v_cvt_pk_bf16_f32 v63, v54, v55\n"
    "v_cvt_pk_bf16_f32 v64, v56, v57\n"
    "v_cvt_pk_bf16_f32 v65, v58, v59\n"
    "ds_write_b64 v6, v[60:61] offset:0\n"
    "ds_write_b64 v6, v[62:63] offset:32\n"
    "ds_write_b64 v6, v[64:65] offset:64\n"
    "ds_read_b128 v[60:63], v7 offset:0\n"
    "ds_read_b128 v[64:67], v7 offset:896\n"
    "s_waitcnt lgkmcnt(0)\n"
    "buffer_store_dwordx4 v[60:63], v4, s[44:47], s81 offen sc1\n"
    "s_add_i32 s73, s81, s62\n"
    "buffer_store_dwordx4 v[64:67], v4, s[44:47], s73 offen sc1\n"
    "s_waitcnt vmcnt(2)\n"
    "v_add_u32 v16, v16, v11\n"
    "v_cmp_gt_i32 s[92:93], s68, v16\n"
    "v_cmp_gt_i32 vcc, s69, v16\n"
    "s_cmp_eq_u32 vcc_hi, 0\n"
    "s_cselect_b32 s93, 1, 2\n"
    "s_cmp_eq_u32 s92, 0\n"
    "s_cselect_b32 s87, 0, s93\n"
    "v_mov_b32 v17, s87\n"
    "ds_write_b32 v12, v17 offset:16\n"
    "s_waitcnt lgkmcnt(0)\n"
    "s_barrier\n"
    "ds_read_b32 v16, v10 offset:16\n"
    "s_waitcnt lgkmcnt(0)\n"
    "v_readfirstlane_b32 s71, v16\n"
    "s_cmp_lg_u32 s71, 2\n"
    "s_cbranch_scc1 L_go0%=\n"
    "s_waitcnt vmcnt(0)\n"
    "s_barrier\n"
    "s_mov_b64 exec, s[90:91]\n"
    "buffer_store_dword v19, off, s[44:47], s82 sc1\n"
    "s_mov_b64 exec, -1\n"
    "s_mov_b32 s46, 0\n"
    "s_cmp_ge_u32 s72, 128\n"
    "s_cbranch_scc1 L_exit%=\n"
    "L_start%=:\n"
    "s_cmp_lg_u32 s65, 0\n"
    "s_cbranch_scc1 L_got0%=\n"
    "L_poll0%=:\n"
    "global_load_dword v16, v3, s[52:53] sc1\n"
    "s_waitcnt vmcnt(0)\n"
    "v_add_u32 v16, v16, v11\n"
    "v_cmp_gt_i32 s[92:93], s68, v16\n"
    "v_cmp_gt_i32 vcc, s69, v16\n"
    "s_cmp_eq_u32 vcc_hi, 0\n"
    "s_cselect_b32 s93, 1, 2\n"
    "s_cmp_eq_u32 s92, 0\n"
    "s_cselect_b32 s87, 0, s93\n"
    "s_cmp_lg_u32 s87, 2\n"
    "s_cbranch_scc1 L_rdy0%=\n"
    "s_memrealtime s[88:89]\n"
    "s_waitcnt lgkmcnt(0)\n"
    "s_sub_i32 s74, s64, s88\n"
    "s_cmp_ge_u32 s68, 64\n"
    "s_cselect_b32 s87, 1, 0\n"
    "s_cmp_lt_i32 s74, 0\n"
    "s_cbranch_scc1 L_rdy0%=\n"
    "s_sleep 2\n"
    "s_branch L_poll0%=\n"
    "L_rdy0%=:\n"
    "v_mov_b32 v17, s87\n"
    "ds_write_b32 v10, v17 offset:32\n"
    "s_waitcnt lgkmcnt(0)\n"
    "L_got0%=:\n"
    "s_barrier\n"
    "ds_read_b32 v16, v10 offset:32\n"
    "s_waitcnt lgkmcnt(0)\n"
    "v_readfirstlane_b32 s71, v16\n"
    "L_go0%=:\n"
    "s_cmp_eq_u32 s71, 1\n"
    "s_cselect_b32 s80, s69, s68\n"
    "s_cselect_b32 s86, s55, s54\n"
    "s_cselect_b32 s75, s57, s56\n"
    "s_and_b32 s87, s80, 7\n"
    "s_mul_i32 s87, s87, s58\n"
    "s_add_i32 s86, s86, s87\n"
    "s_add_i32 m0, s66, 0\n"
    "s_add_i32 s74, s86, 0\n"
    "buffer_load_dwordx4 v0, s[36:39], s74 offen offset:0 sc1 lds\n"
    "buffer_load_dwordx4 v0, s[36:39], s74 offen offset:1024 sc1 lds\n"
    "buffer_load_dwordx4 v0, s[36:39], s74 offen offset:2048 sc1 lds\n"
    "buffer_load_dwordx4 v0, s[36:39], s74 offen offset:3072 sc1 lds\n"
    "s_add_i32 m0, s66, 16384\n"
    "s_add_i32 s74, s86, 32768\n"
    "buffer_load_dwordx4 v0, s[36:39], s74 offen offset:0 sc1 lds\n"
    "buffer_load_dwordx4 v0, s[36:39], s74 offen offset:1024 sc1 lds\n"
    "buffer_load_dwordx4 v0, s[36:39], s74 offen offset:2048 sc1 lds\n"
    "buffer_load_dwordx4 v0, s[36:39], s74 offen offset:3072 sc1 lds\n"
    "s_and_b32 s87, s80, 7\n"
    "s_mul_i32 s87, s87, s59\n"
    "s_add_i32 s75, s75, s87\n"
    "s_and_b32 s92, s71, 1\n"
    "s_add_i32 s69, s69, s92\n"
    "s_lshr_b32 s92, 1, s71\n"
    "s_add_i32 s68, s68, s92\n"
    "s_mov_b32 s77, s80\n"
    "s_mul_i32 s76, s71, s94\n"
    "s_add_i32 s76, s76, s60\n"
    "s_waitcnt vmcnt(4)\n"
    "s_barrier\n"
    "ds_read_b128 v[24:27], v1 offset:1024\n"
    "ds_read_b128 v[20:23], v1 offset:0\n"
    "global_load_dword v16, v3, s[52:53] sc1\n"
    "s_mov_b64 exec, s[90:91]\n"
    "buffer_store_dword v19, off, s[44:47], s82 sc1\n"
    "s_mov_b64 exec, -1\n"
    "s_mov_b32 s46, 0\n"
    "s_branch L_body0%=\n"
    "L_idle1%=:\n"
    "s_nop 4\n"
    "v_mov_b32 v19, s83\n"
    "v_cvt_pk_bf16_f32 v60, v36, v37\n"
    "v_cvt_pk_bf16_f32 v61, v38, v39\n"
    "v_cvt_pk_bf16_f32 v62, v40, v41\n"
    "v_cvt_pk_bf16_f32 v63, v42, v43\n"
    "v_cvt_pk_bf16_f32 v64, v44, v45\n"
    "v_cvt_pk_bf16_f32 v65, v46, v47\n"
    "ds_write_b64 v6, v[60:61] offset:0\n"
    "ds_write_b64 v6, v[62:63] offset:32\n"
    "ds_write_b64 v6, v[64:65] offset:64\n"
    "ds_read_b128 v[60:63], v7 offset:0\n"
    "ds_read_b128 v[64:67], v7 offset:896\n"
    "s_waitcnt lgkmcnt(0)\n"
    "buffer_store_dwordx4 v[60:63], v4, s[44:47], s81 offen sc1\n"
    "s_add_i32 s73, s81, s62\n"
    "buffer_store_dwordx4 v[64:67], v4, s[44:47], s73 offen sc1\n"
    "s_waitcnt vmcnt(2)\n"
    "v_add_u32 v16, v16, v11\n"
    "v_cmp_gt_i32 s[92:93], s68, v16\n"
    "v_cmp_gt_i32 vcc, s69, v16\n"
    "s_cmp_eq_u32 vcc_hi, 0\n"
    "s_cselect_b32 s93, 1, 2\n"
    "s_cmp_eq_u32 s92, 0\n"
    "s_cselect_b32 s87, 0, s93\n"
    "v_mov_b32 v17, s87\n"
    "ds_write_b32 v12, v17 offset:16\n"
    "s_waitcnt lgkmcnt(0)\n"
    "s_barrier\n"
    "ds_read_b32 v16, v10 offset:16\n"
    "s_waitcnt lgkmcnt(0)\n"
    "v_readfirstlane_b32 s71, v16\n"
    "s_cmp_lg_u32 s71, 2\n"
    "s_cbranch_scc1 L_go1%=\n"
    "s_waitcnt vmcnt(0)\n"
    "s_barrier\n"
    "s_mov_b64 exec, s[90:91]\n"
    "buffer_store_dword v19, off, s[44:47], s82 sc1\n"
    "s_mov_b64 exec, -1\n"
    "s_mov_b32 s46, 0\n"
    "s_cmp_lg_u32 s65, 0\n"
    "s_cbranch_scc1 L_got1%=\n"
    "L_poll1%=:\n"
    "global_load_dword v16, v3, s[52:53] sc1\n"
    "s_waitcnt vmcnt(0)\n"
    "v_add_u32 v16, v16, v11\n"
    "v_cmp_gt_i32 s[92:93], s68, v16\n"
    "v_cmp_gt_i32 vcc, s69, v16\n"
    "s_cmp_eq_u32 vcc_hi, 0\n"
    "s_cselect_b32 s93, 1, 2\n"
    "s_cmp_eq_u32 s92, 0\n"
    "s_cselect_b32 s87, 0, s93\n"
    "s_cmp_lg_u32 s87, 2\n"
    "s_cbranch_scc1 L_rdy1%=\n"
    "s_memrealtime s[88:89]\n"
    "s_waitcnt lgkmcnt(0)\n"
    "s_sub_i32 s74, s64, s88\n"
    "s_cmp_ge_u32 s68, 64\n"
    "s_cselect_b32 s87, 1, 0\n"
    "s_cmp_lt_i32 s74, 0\n"
    "s_cbranch_scc1 L_rdy1%=\n"
    "s_sleep 2\n"
    "s_branch L_poll1%=\n"
    "L_rdy1%=:\n"
    "v_mov_b32 v17, s87\n"
    "ds_write_b32 v10, v17 offset:32\n"
    "s_waitcnt lgkmcnt(0)\n"
    "L_got1%=:\n"
    "s_barrier\n"
    "ds_read_b32 v16, v10 offset:32\n"
    "s_waitcnt lgkmcnt(0)\n"
    "v_readfirstlane_b32 s71, v16\n"
    "L_go1%=:\n"
    "s_cmp_eq_u32 s71, 1\n"
    "s_cselect_b32 s80, s69, s68\n"
    "s_cselect_b32 s86, s55, s54\n"
    "s_cselect_b32 s75, s57, s56\n"
    "s_and_b32 s87, s80, 7\n"
    "s_mul_i32 s87, s87, s58\n"
    "s_add_i32 s86, s86, s87\n"
    "s_add_i32 m0, s66, 32768\n"
    "s_add_i32 s74, s86, 0\n"
    "buffer_load_dwordx4 v0, s[36:39], s74 offen offset:0 sc1 lds\n"
    "buffer_load_dwordx4 v0, s[36:39], s74 offen offset:1024 sc1 lds\n"
    "buffer_load_dwordx4 v0, s[36:39], s74 offen offset:2048 sc1 lds\n"
    "buffer_load_dwordx4 v0, s[36:39], s74 offen offset:3072 sc1 lds\n"
    "s_add_i32 m0, s66, 49152\n"
    "s_add_i32 s74, s86, 32768\n"
    "buffer_load_dwordx4 v0, s[36:39], s74 offen offset:0 sc1 lds\n"
    "buffer_load_dwordx4 v0, s[36:39], s74 offen offset:1024 sc1 lds\n"
    "buffer_load_dwordx4 v0, s[36:39], s74 offen offset:2048 sc1 lds\n"
    "buffer_load_dwordx4 v0, s[36:39], s74 offen offset:3072 sc1 lds\n"
    "s_and_b32 s87, s80, 7\n"
    "s_mul_i32 s87, s87, s59\n"
    "s_add_i32 s75, s75, s87\n"
    "s_and_b32 s92, s71, 1\n"
    "s_add_i32 s69, s69, s92\n"
    "s_lshr_b32 s92, 1, s71\n"
    "s_add_i32 s68, s68, s92\n"
    "s_mov_b32 s77, s80\n"
    "s_mul_i32 s76, s71, s94\n"
    "s_add_i32 s76, s76, s60\n"
    "s_waitcnt vmcnt(4)\n"
    "s_barrier\n"
    "ds_read_b128 v[24:27], v8 offset:1024\n"
    "ds_read_b128 v[20:23], v8 offset:0\n"
    "global_load_dword v16, v3, s[52:53] sc1\n"
    "s_mov_b64 exec, s[90:91]\n"
    "buffer_store_dword v19, off, s[44:47], s82 sc1\n"
    "s_mov_b64 exec, -1\n"
    "s_mov_b32 s46, 0\n"
    "s_branch L_body1%=\n"
    "L_exit%=:\n"
    : GEMM_OUT : GEMM_IN, GEMM_IN3 : PART_CLOBBERS);
  if constexpr (MODE == M_DN) asm volatile(
    "s_nop 4\n"
    "s_mov_b32 s40, s36\n"
    "s_mov_b32 s41, s37\n"
    "s_mov_b32 s42, 0\n"
    "s_mov_b32 s43, s39\n"
    "s_mov_b32 s44, s36\n"
    "s_mov_b32 s45, s37\n"
    "s_mov_b32 s46, 0\n"
    "s_mov_b32 s47, s39\n"
    "s_mov_b32 s68, 0\n"
    "s_mov_b32 s69, 0\n"
    "s_mov_b32 s70, 0\n"
    "s_mov_b32 s72, 0\n"
    "s_mov_b32 s81, 0\n"
    "s_mov_b32 s82, 0\n"
    "s_mov_b32 s83, 0\n"
    "s_mov_b32 s91, 0\n"
    "s_lshr_b32 s90, 1, s65\n"
    "s_lshl_b32 s73, s65, 2\n"
    "v_add_u32 v12, s73, v10\n"
    "v_mov_b32 v18, 0xbdd2d3e8\n"
    "s_branch L_start%=\n"
    "L_body0%=:\n"
    "s_waitcnt lgkmcnt(0)\n"
    "v_mfma_f32_16x16x32_bf16 v[36:39], a[0:3], v[20:23], 0\n"
    "ds_read_b128 v[32:35], v1 offset:3072\n"
    "v_mov_b32 v19, s83\n"
    "v_mfma_f32_16x16x32_bf16 v[36:39], a[4:7], v[24:27], v[36:39]\n"
    "ds_read_b128 v[28:31], v1 offset:2048\n"
    "v_mfma_f32_16x16x32_bf16 v[40:43], a[144:147], v[20:23], 0\n"
    "v_cvt_pk_bf16_f32 v60, v48, v49\n"
    "v_cvt_pk_bf16_f32 v61, v50, v51\n"
    "v_mfma_f32_16x16x32_bf16 v[40:43], a[148:151], v[24:27], v[40:43]\n"
    "v_cvt_pk_bf16_f32 v62, v52, v53\n"
    "v_cvt_pk_bf16_f32 v63, v54, v55\n"
    "v_mfma_f32_16x16x32_bf16 v[44:47], v[112:115], v[20:23], 0\n"
    "v_cvt_pk_bf16_f32 v64, v56, v57\n"
    "v_cvt_pk_bf16_f32 v65, v58, v59\n"
    "v_mfma_f32_16x16x32_bf16 v[44:47], v[116:119], v[24:27], v[44:47]\n"
    "s_waitcnt lgkmcnt(0)\n"
    "ds_write_b64 v6, v[60:61] offset:0\n"
    "ds_write_b64 v6, v[62:63] offset:32\n"
    "v_mfma_f32_16x16x32_bf16 v[36:39], a[8:11], v[28:31], v[36:39]\n"
    "ds_read_b128 v[24:27], v1 offset:5120\n"
    "ds_write_b64 v6, v[64:65] offset:64\n"
    "v_mfma_f32_16x16x32_bf16 v[36:39], a[12:15], v[32:35], v[36:39]\n"
    "ds_read_b128 v[20:23], v1 offset:4096\n"
    "ds_read_b128 v[60:63], v7 offset:0\n"
    "v_mfma_f32_16x16x32_bf16 v[40:43], a[152:155], v[28:31], v[40:43]\n"
    "ds_read_b128 v[64:67], v7 offset:896\n"
    "v_mfma_f32_16x16x32_bf16 v[40:43], a[156:159], v[32:35], v[40:43]\n"
    "v_mfma_f32_16x16x32_bf16 v[44:47], v[120:123], v[28:31], v[44:47]\n"
    "v_mfma_f32_16x16x32_bf16 v[44:47], v[124:127], v[32:35], v[44:47]\n"
    "s_waitcnt lgkmcnt(2)\n"
    "v_mfma_f32_16x16x32_bf16 v[36:39], a[16:19], v[20:23], v[36:39]\n"
    "s_waitcnt lgkmcnt(1)\n"
    "ds_read_b128 v[32:35], v1 offset:7168\n"
    "buffer_store_dwordx4 v[60:63], v4, s[44:47], s81 offen sc1\n"
    "v_mfma_f32_16x16x32_bf16 v[36:39], a[20:23], v[24:27], v[36:39]\n"
    "s_add_i32 s73, s81, s62\n"
    "ds_read_b128 v[28:31], v1 offset:6144\n"
    "v_mfma_f32_16x16x32_bf16 v[40:43], a[160:163], v[20:23], v[40:43]\n"
    "v_mfma_f32_16x16x32_bf16 v[40:43], a[164:167], v[24:27], v[40:43]\n"
    "v_mfma_f32_16x16x32_bf16 v[44:47], v[128:131], v[20:23], v[44:47]\n"
    "v_mfma_f32_16x16x32_bf16 v[44:47], v[132:135], v[24:27], v[44:47]\n"
    "s_waitcnt lgkmcnt(0)\n"
    "s_waitcnt lgkmcnt(2)\n"
    "buffer_store_dwordx4 v[64:67], v4, s[44:47], s73 offen sc1\n"
    "v_mfma_f32_16x16x32_bf16 v[36:39], a[24:27], v[28:31], v[36:39]\n"
    "ds_read_b128 v[24:27], v1 offset:9216\n"
    "v_mfma_f32_16x16x32_bf16 v[36:39], a[28:31], v[32:35], v[36:39]\n"
    "ds_read_b128 v[20:23], v1 offset:8192\n"
    "v_mfma_f32_16x16x32_bf16 v[40:43], a[168:171], v[28:31], v[40:43]\n"
    "s_waitcnt vmcnt(2)\n"
    "v_add_u32 v16, v16, v11\n"
    "v_mfma_f32_16x16x32_bf16 v[40:43], a[172:175], v[32:35], v[40:43]\n"
    "v_cmp_gt_i32 s[92:93], s68, v16\n"
    "v_mfma_f32_16x16x32_bf16 v[44:47], v[136:139], v[28:31], v[44:47]\n"
    "v_cmp_gt_i32 vcc, s69, v16\n"
    "v_mfma_f32_16x16x32_bf16 v[44:47], v[140:143], v[32:35], v[44:47]\n"
    "s_waitcnt lgkmcnt(0)\n"
    "v_mfma_f32_16x16x32_bf16 v[36:39], a[32:35], v[20:23], v[36:39]\n"
    "s_cmp_eq_u32 vcc_hi, 0\n"
    "s_cselect_b32 s93, 1, 2\n"
    "ds_read_b128 v[32:35], v1 offset:11264\n"
    "v_mfma_f32_16x16x32_bf16 v[36:39], a[36:39], v[24:27], v[36:39]\n"
    "s_cmp_eq_u32 s92, 0\n"
    "s_cselect_b32 s87, 0, s93\n"
    "ds_read_b128 v[28:31], v1 offset:10240\n"
    "v_mfma_f32_16x16x32_bf16 v[40:43], a[176:179], v[20:23], v[40:43]\n"
    "v_mov_b32 v17, s87\n"
    "v_mfma_f32_16x16x32_bf16 v[40:43], a[180:183], v[24:27], v[40:43]\n"
    "ds_write_b32 v12, v17\n"
    "v_mfma_f32_16x16x32_bf16 v[44:47], v[144:147], v[20:23], v[44:47]\n"
    "v_mfma_f32_16x16x32_bf16 v[44:47], v[148:151], v[24:27], v[44:47]\n"
    "s_waitcnt lgkmcnt(1)\n"
    "v_mfma_f32_16x16x32_bf16 v[36:39], a[40:43], v[28:31], v[36:39]\n"
    "s_waitcnt lgkmcnt(0)\n"
    "s_barrier\n"
    "ds_read_b32 v16, v10\n"
    "v_mfma_f32_16x16x32_bf16 v[36:39], a[44:47], v[32:35], v[36:39]\n"
    "ds_read_b128 v[24:27], v1 offset:13312\n"
    "ds_read_b128 v[20:23], v1 offset:12288\n"
    "v_mfma_f32_16x16x32_bf16 v[40:43], a[184:187], v[28:31], v[40:43]\n"
    "v_mfma_f32_16x16x32_bf16 v[40:43], a[188:191], v[32:35], v[40:43]\n"
    "v_mfma_f32_16x16x32_bf16 v[44:47], v[152:155], v[28:31], v[44:47]\n"
    "s_waitcnt lgkmcnt(2)\n"
    "v_readfirstlane_b32 s71, v16\n"
    "v_mfma_f32_16x16x32_bf16 v[44:47], v[156:159], v[32:35], v[44:47]\n"
    "s_waitcnt lgkmcnt(0)\n"
    "s_cmp_lg_u32 s71, 2\n"
    "s_cselect_b32 s42, s38, 0\n"
    "v_mfma_f32_16x16x32_bf16 v[36:39], a[48:51], v[20:23], v[36:39]\n"
    "s_cmp_eq_u32 s71, 1\n"
    "s_cselect_b32 s80, s69, s68\n"
    "ds_read_b128 v[32:35], v1 offset:15360\n"
    "v_mfma_f32_16x16x32_bf16 v[36:39], a[52:55], v[24:27], v[36:39]\n"
    "s_cselect_b32 s86, s55, s54\n"
    "s_cselect_b32 s78, s57, s56\n"
    "ds_read_b128 v[28:31], v1 offset:14336\n"
    "v_mfma_f32_16x16x32_bf16 v[40:43], a[192:195], v[20:23], v[40:43]\n"
    "s_and_b32 s87, s80, 7\n"
    "s_mul_i32 s87, s87, s58\n"
    "s_add_i32 s86, s86, s87\n"
    "v_mfma_f32_16x16x32_bf16 v[40:43], a[196:199], v[24:27], v[40:43]\n"
    "v_mfma_f32_16x16x32_bf16 v[44:47], v[160:163], v[20:23], v[44:47]\n"
    "v_mfma_f32_16x16x32_bf16 v[44:47], v[164:167], v[24:27], v[44:47]\n"
    "s_waitcnt lgkmcnt(0)\n"
    "s_add_i32 m0, s66, 36864\n"
    "s_add_i32 s74, s86, 0\n"
    "v_mfma_f32_16x16x32_bf16 v[36:39], a[56:59], v[28:31], v[36:39]\n"
    "ds_read_b128 v[24:27], v1 offset:17408\n"
    "buffer_load_dwordx4 v0, s[40:43], s74 offen offset:0 sc1 lds\n"
    "v_mfma_f32_16x16x32_bf16 v[36:39], a[60:63], v[32:35], v[36:39]\n"
    "ds_read_b128 v[20:23], v1 offset:16384\n"
    "v_mfma_f32_16x16x32_bf16 v[40:43], a[200:203], v[28:31], v[40:43]\n"
    "s_and_b32 s87, s80, 7\n"
    "s_mul_i32 s87, s87, s59\n"
    "s_add_i32 s78, s78, s87\n"
    "v_mfma_f32_16x16x32_bf16 v[40:43], a[204:207], v[32:35], v[40:43]\n"
    "s_and_b32 s92, s71, 1\n"
    "s_add_i32 s69, s69, s92\n"
    "s_lshr_b32 s92, 1, s71\n"
    "v_mfma_f32_16x16x32_bf16 v[44:47], v[168:171], v[28:31], v[44:47]\n"
    "s_add_i32 s68, s68, s92\n"
    "buffer_load_dwordx4 v0, s[40:43], s74 offen offset:1024 sc1 lds\n"
    "v_mfma_f32_16x16x32_bf16 v[44:47], v[172:175], v[32:35], v[44:47]\n"
    "s_waitcnt lgkmcnt(0)\n"
    "v_mfma_f32_16x16x32_bf16 v[36:39], a[64:67], v[20:23], v[36:39]\n"
    "ds_read_b128 v[32:35], v1 offset:19456\n"
    "v_mfma_f32_16x16x32_bf16 v[36:39], a[68:71], v[24:27], v[36:39]\n"
    "ds_read_b128 v[28:31], v1 offset:18432\n"
    "v_mfma_f32_16x16x32_bf16 v[40:43], a[208:211], v[20:23], v[40:43]\n"
    "buffer_load_dwordx4 v0, s[40:43], s74 offen offset:2048 sc1 lds\n"
    "v_mfma_f32_16x16x32_bf16 v[40:43], a[212:215], v[24:27], v[40:43]\n"
    "v_mfma_f32_16x16x32_bf16 v[44:47], v[176:179], v[20:23], v[44:47]\n"
    "v_mfma_f32_16x16x32_bf16 v[44:47], v[180:183], v[24:27], v[44:47]\n"
    "s_waitcnt lgkmcnt(0)\n"
    "v_mfma_f32_16x16x32_bf16 v[36:39], a[72:75], v[28:31], v[36:39]\n"
    "ds_read_b128 v[24:27], v1 offset:21504\n"
    "buffer_load_dwordx4 v0, s[40:43], s74 offen offset:3072 sc1 lds\n"
    "v_mfma_f32_16x16x32_bf16 v[36:39], a[76:79], v[32:35], v[36:39]\n"
    "ds_read_b128 v[20:23], v1 offset:20480\n"
    "v_mfma_f32_16x16x32_bf16 v[40:43], a[216:219], v[28:31], v[40:43]\n"
    "v_mfma_f32_16x16x32_bf16 v[40:43], a[220:223], v[32:35], v[40:43]\n"
    "v_mfma_f32_16x16x32_bf16 v[44:47], v[184:187], v[28:31], v[44:47]\n"
    "v_mfma_f32_16x16x32_bf16 v[44:47], v[188:191], v[32:35], v[44:47]\n"
    "s_waitcnt lgkmcnt(0)\n"
    "s_add_i32 m0, s67, 53248\n"
    "s_add_i32 s74, s86, 65536\n"
    "v_mfma_f32_16x16x32_bf16 v[36:39], a[80:83], v[20:23], v[36:39]\n"
    "ds_read_b128 v[32:35], v1 offset:23552\n"
    "buffer_load_dwordx4 v9, s[40:43], s74 offen offset:0 sc1 lds\n"
    "v_mfma_f32_16x16x32_bf16 v[36:39], a[84:87], v[24:27], v[36:39]\n"
    "s_waitcnt vmcnt(5)\n"
    "s_barrier\n"
    "ds_read_b128 v[28:31], v1 offset:22528\n"
    "v_mfma_f32_16x16x32_bf16 v[40:43], a[224:227], v[20:23], v[40:43]\n"
    "s_mov_b64 exec, s[90:91]\n"
    "buffer_store_dword v19, off, s[44:47], s82 sc1\n"
    "s_mov_b64 exec, -1\n"
    "v_mfma_f32_16x16x32_bf16 v[40:43], a[228:231], v[24:27], v[40:43]\n"
    "s_mov_b32 s46, s38\n"
    "s_mov_b32 s81, s75\n"
    "s_mov_b32 s82, s76\n"
    "v_mfma_f32_16x16x32_bf16 v[44:47], v[192:195], v[20:23], v[44:47]\n"
    "s_mov_b32 s83, s77\n"
    "s_mov_b32 s75, s78\n"
    "s_mov_b32 s77, s80\n"
    "v_mfma_f32_16x16x32_bf16 v[44:47], v[196:199], v[24:27], v[44:47]\n"
    "s_waitcnt lgkmcnt(0)\n"
    "s_add_i32 m0, s66, 57344\n"
    "s_add_i32 s74, s86, 32768\n"
    "v_mfma_f32_16x16x32_bf16 v[36:39], a[88:91], v[28:31], v[36:39]\n"
    "s_mul_i32 s76, s71, s94\n"
    "ds_read_b128 v[24:27], v1 offset:25600\n"
    "buffer_load_dwordx4 v0, s[40:43], s74 offen offset:0 sc1 lds\n"
    "v_mfma_f32_16x16x32_bf16 v[36:39], a[92:95], v[32:35], v[36:39]\n"
    "s_add_i32 s76, s76, s60\n"
    "ds_read_b128 v[20:23], v1 offset:24576\n"
    "v_mfma_f32_16x16x32_bf16 v[40:43], a[232:235], v[28:31], v[40:43]\n"
    "v_mfma_f32_16x16x32_bf16 v[40:43], a[236:239], v[32:35], v[40:43]\n"
    "v_mfma_f32_16x16x32_bf16 v[44:47], v[200:203], v[28:31], v[44:47]\n"
    "v_mfma_f32_16x16x32_bf16 v[44:47], v[204:207], v[32:35], v[44:47]\n"
    "s_waitcnt lgkmcnt(0)\n"
    "v_mfma_f32_16x16x32_bf16 v[36:39], a[96:99], v[20:23], v[36:39]\n"
    "ds_read_b128 v[32:35], v1 offset:27648\n"
    "buffer_load_dwordx4 v0, s[40:43], s74 offen offset:1024 sc1 lds\n"
    "v_mfma_f32_16x16x32_bf16 v[36:39], a[100:103], v[24:27], v[36:39]\n"
    "ds_read_b128 v[28:31], v1 offset:26624\n"
    "v_mfma_f32_16x16x32_bf16 v[40:43], a[240:243], v[20:23], v[40:43]\n"
    "v_mfma_f32_16x16x32_bf16 v[40:43], a[244:247], v[24:27], v[40:43]\n"
    "v_mfma_f32_16x16x32_bf16 v[44:47], v[208:211], v[20:23], v[44:47]\n"
    "v_mfma_f32_16x16x32_bf16 v[44:47], v[212:215], v[24:27], v[44:47]\n"
    "s_waitcnt lgkmcnt(0)\n"
    "v_mfma_f32_16x16x32_bf16 v[36:39], a[104:107], v[28:31], v[36:39]\n"
    "ds_read_b128 v[24:27], v1 offset:29696\n"
    "buffer_load_dwordx4 v0, s[40:43], s74 offen offset:2048 sc1 lds\n"
    "v_mfma_f32_16x16x32_bf16 v[36:39], a[108:111], v[32:35], v[36:39]\n"
    "ds_read_b128 v[20:23], v1 offset:28672\n"
    "v_mfma_f32_16x16x32_bf16 v[40:43], a[248:251], v[28:31], v[40:43]\n"
    "v_mfma_f32_16x16x32_bf16 v[40:43], a[252:255], v[32:35], v[40:43]\n"
    "v_mfma_f32_16x16x32_bf16 v[44:47], v[216:219], v[28:31], v[44:47]\n"
    "v_mfma_f32_16x16x32_bf16 v[44:47], v[220:223], v[32:35], v[44:47]\n"
    "s_waitcnt lgkmcnt(0)\n"
    "v_mfma_f32_16x16x32_bf16 v[36:39], a[112:115], v[20:23], v[36:39]\n"
    "ds_read_b128 v[32:35], v1 offset:31744\n"
    "buffer_load_dwordx4 v0, s[40:43], s74 offen offset:3072 sc1 lds\n"
    "v_mfma_f32_16x16x32_bf16 v[36:39], a[116:119], v[24:27], v[36:39]\n"
    "ds_read_b128 v[28:31], v1 offset:30720\n"
    "v_mfma_f32_16x16x32_bf16 v[40:43], v[80:83], v[20:23], v[40:43]\n"
    "v_mfma_f32_16x16x32_bf16 v[40:43], v[84:87], v[24:27], v[40:43]\n"
    "v_mfma_f32_16x16x32_bf16 v[44:47], v[224:227], v[20:23], v[44:47]\n"
    "v_mfma_f32_16x16x32_bf16 v[44:47], v[228:231], v[24:27], v[44:47]\n"
    "s_waitcnt lgkmcnt(0)\n"
    "v_mfma_f32_16x16x32_bf16 v[36:39], a[120:123], v[28:31], v[36:39]\n"
    "ds_read_b128 v[24:27], v1 offset:33792\n"
    "v_mfma_f32_16x16x32_bf16 v[36:39], a[124:127], v[32:35], v[36:39]\n"
    "ds_read_b128 v[20:23], v1 offset:32768\n"
    "v_mfma_f32_16x16x32_bf16 v[40:43], v[88:91], v[28:31], v[40:43]\n"
    "v_mfma_f32_16x16x32_bf16 v[40:43], v[92:95], v[32:35], v[40:43]\n"
    "v_mfma_f32_16x16x32_bf16 v[44:47], v[232:235], v[28:31], v[44:47]\n"
    "v_mfma_f32_16x16x32_bf16 v[44:47], v[236:239], v[32:35], v[44:47]\n"
    "s_waitcnt lgkmcnt(0)\n"
    "v_mfma_f32_16x16x32_bf16 v[36:39], a[128:131], v[20:23], v[36:39]\n"
    "ds_read_b128 v[32:35], v1 offset:35840\n"
    "global_load_dword v16, v3, s[52:53] sc1\n"
    "v_mfma_f32_16x16x32_bf16 v[36:39], a[132:135], v[24:27], v[36:39]\n"
    "ds_read_b128 v[28:31], v1 offset:34816\n"
    "v_mfma_f32_16x16x32_bf16 v[40:43], v[96:99], v[20:23], v[40:43]\n"
    "v_mfma_f32_16x16x32_bf16 v[40:43], v[100:103], v[24:27], v[40:43]\n"
    "v_mfma_f32_16x16x32_bf16 v[44:47], v[240:243], v[20:23], v[44:47]\n"
    "v_mfma_f32_16x16x32_bf16 v[44:47], v[244:247], v[24:27], v[44:47]\n"
    "s_waitcnt lgkmcnt(0)\n"
    "v_mfma_f32_16x16x32_bf16 v[36:39], a[136:139], v[28:31], v[36:39]\n"
    "v_mfma_f32_16x16x32_bf16 v[36:39], a[140:143], v[32:35], v[36:39]\n"
    "s_waitcnt vmcnt(6) lgkmcnt(0)\n"
    "s_barrier\n"
    "ds_read_b128 v[24:27], v8 offset:1024\n"
    "v_mfma_f32_16x16x32_bf16 v[40:43], v[104:107], v[28:31], v[40:43]\n"
    "ds_read_b128 v[20:23], v8 offset:0\n"
    "v_mfma_f32_16x16x32_bf16 v[40:43], v[108:111], v[32:35], v[40:43]\n"
    "v_mfma_f32_16x16x32_bf16 v[44:47], v[248:251], v[28:31], v[44:47]\n"
    "v_mfma_f32_16x16x32_bf16 v[44:47], v[252:255], v[32:35], v[44:47]\n"
    "s_cmp_eq_u32 s42, 0\n"
    "s_cbranch_scc1 L_idle1%=\n"
    "L_body1%=:\n"
    "s_waitcnt lgkmcnt(0)\n"
    "v_mfma_f32_16x16x32_bf16 v[48:51], a[0:3], v[20:23], 0\n"
    "ds_read_b128 v[32:35], v8 offset:3072\n"
    "v_mov_b32 v19, s83\n"
    "v_mfma_f32_16x16x32_bf16 v[48:51], a[4:7], v[24:27], v[48:51]\n"
    "ds_read_b128 v[28:31], v8 offset:2048\n"
    "v_mfma_f32_16x16x32_bf16 v[52:55], a[144:147], v[20:23], 0\n"
    "v_cvt_pk_bf16_f32 v60, v36, v37\n"
    "v_cvt_pk_bf16_f32 v61, v38, v39\n"
    "v_mfma_f32_16x16x32_bf16 v[52:55], a[148:151], v[24:27], v[52:55]\n"
    "v_cvt_pk_bf16_f32 v62, v40, v41\n"
    "v_cvt_pk_bf16_f32 v63, v42, v43\n"
    "v_mfma_f32_16x16x32_bf16 v[56:59], v[112:115], v[20:23], 0\n"
    "v_cvt_pk_bf16_f32 v64, v44, v45\n"
    "v_cvt_pk_bf16_f32 v65, v46, v47\n"
    "v_mfma_f32_16x16x32_bf16 v[56:59], v[116:119], v[24:27], v[56:59]\n"
    "s_waitcnt lgkmcnt(0)\n"
    "ds_write_b64 v6, v[60:61] offset:0\n"
    "ds_write_b64 v6, v[62:63] offset:32\n"
    "v_mfma_f32_16x16x32_bf16 v[48:51], a[8:11], v[28:31], v[48:51]\n"
    "ds_read_b128 v[24:27], v8 offset:5120\n"
    "ds_write_b64 v6, v[64:65] offset:64\n"
    "v_mfma_f32_16x16x32_bf16 v[48:51], a[12:15], v[32:35], v[48:51]\n"
    "ds_read_b128 v[20:23], v8 offset:4096\n"
    "ds_read_b128 v[60:63], v7 offset:0\n"
    "v_mfma_f32_16x16x32_bf16 v[52:55], a[152:155], v[28:31], v[52:55]\n"
    "ds_read_b128 v[64:67], v7 offset:896\n"
    "v_mfma_f32_16x16x32_bf16 v[52:55], a[156:159], v[32:35], v[52:55]\n"
    "v_mfma_f32_16x16x32_bf16 v[56:59], v[120:123], v[28:31], v[56:59]\n"
    "v_mfma_f32_16x16x32_bf16 v[56:59], v[124:127], v[32:35], v[56:59]\n"
    "s_waitcnt lgkmcnt(2)\n"
    "v_mfma_f32_16x16x32_bf16 v[48:51], a[16:19], v[20:23], v[48:51]\n"
    "s_waitcnt lgkmcnt(1)\n"
    "ds_read_b128 v[32:35], v8 offset:7168\n"
    "buffer_store_dwordx4 v[60:63], v4, s[44:47], s81 offen sc1\n"
    "v_mfma_f32_16x16x32_bf16 v[48:51], a[20:23], v[24:27], v[48:51]\n"
    "s_add_i32 s73, s81, s62\n"
    "ds_read_b128 v[28:31], v8 offset:6144\n"
    "v_mfma_f32_16x16x32_bf16 v[52:55], a[160:163], v[20:23], v[52:55]\n"
    "v_mfma_f32_16x16x32_bf16 v[52:55], a[164:167], v[24:27], v[52:55]\n"
    "v_mfma_f32_16x16x32_bf16 v[56:59], v[128:131], v[20:23], v[56:59]\n"
    "v_mfma_f32_16x16x32_bf16 v[56:59], v[132:135], v[24:27], v[56:59]\n"
    "s_waitcnt lgkmcnt(0)\n"
    "s_waitcnt lgkmcnt(2)\n"
    "buffer_store_dwordx4 v[64:67], v4, s[44:47], s73 offen sc1\n"
    "v_mfma_f32_16x16x32_bf16 v[48:51], a[24:27], v[28:31], v[48:51]\n"
    "ds_read_b128 v[24:27], v8 offset:9216\n"
    "v_mfma_f32_16x16x32_bf16 v[48:51], a[28:31], v[32:35], v[48:51]\n"
    "ds_read_b128 v[20:23], v8 offset:8192\n"
    "v_mfma_f32_16x16x32_bf16 v[52:55], a[168:171], v[28:31], v[52:55]\n"
    "s_waitcnt vmcnt(2)\n"
    "v_add_u32 v16, v16, v11\n"
    "v_mfma_f32_16x16x32_bf16 v[52:55], a[172:175], v[32:35], v[52:55]\n"
    "v_cmp_gt_i32 s[92:93], s68, v16\n"
    "v_mfma_f32_16x16x32_bf16 v[56:59], v[136:139], v[28:31], v[56:59]\n"
    "v_cmp_gt_i32 vcc, s69, v16\n"
    "v_mfma_f32_16x16x32_bf16 v[56:59], v[140:143], v[32:35], v[56:59]\n"
    "s_waitcnt lgkmcnt(0)\n"
    "v_mfma_f32_16x16x32_bf16 v[48:51], a[32:35], v[20:23], v[48:51]\n"
    "s_cmp_eq_u32 vcc_hi, 0\n"
    "s_cselect_b32 s93, 1, 2\n"
    "ds_read_b128 v[32:35], v8 offset:11264\n"
    "v_mfma_f32_16x16x32_bf16 v[48:51], a[36:39], v[24:27], v[48:51]\n"
    "s_cmp_eq_u32 s92, 0\n"
    "s_cselect_b32 s87, 0, s93\n"
    "ds_read_b128 v[28:31], v8 offset:10240\n"
    "v_mfma_f32_16x16x32_bf16 v[52:55], a[176:179], v[20:23], v[52:55]\n"
    "v_mov_b32 v17, s87\n"
    "v_mfma_f32_16x16x32_bf16 v[52:55], a[180:183], v[24:27], v[52:55]\n"
    "ds_write_b32 v12, v17\n"
    "v_mfma_f32_16x16x32_bf16 v[56:59], v[144:147], v[20:23], v[56:59]\n"
    "v_mfma_f32_16x16x32_bf16 v[56:59], v[148:151], v[24:27], v[56:59]\n"
    "s_waitcnt lgkmcnt(1)\n"
    "v_mfma_f32_16x16x32_bf16 v[48:51], a[40:43], v[28:31], v[48:51]\n"
    "s_waitcnt lgkmcnt(0)\n"
    "s_barrier\n"
    "ds_read_b32 v16, v10\n"
    "v_mfma_f32_16x16x32_bf16 v[48:51], a[44:47], v[32:35], v[48:51]\n"
    "ds_read_b128 v[24:27], v8 offset:13312\n"
    "ds_read_b128 v[20:23], v8 offset:12288\n"
    "v_mfma_f32_16x16x32_bf16 v[52:55], a[184:187], v[28:31], v[52:55]\n"
    "v_mfma_f32_16x16x32_bf16 v[52:55], a[188:191], v[32:35], v[52:55]\n"
    "v_mfma_f32_16x16x32_bf16 v[56:59], v[152:155], v[28:31], v[56:59]\n"
    "s_waitcnt lgkmcnt(2)\n"
    "v_readfirstlane_b32 s71, v16\n"
    "v_mfma_f32_16x16x32_bf16 v[56:59], v[156:159], v[32:35], v[56:59]\n"
    "s_waitcnt lgkmcnt(0)\n"
    "s_cmp_lg_u32 s71, 2\n"
    "s_cselect_b32 s42, s38, 0\n"
    "v_mfma_f32_16x16x32_bf16 v[48:51], a[48:51], v[20:23], v[48:51]\n"
    "s_cmp_eq_u32 s71, 1\n"
    "s_cselect_b32 s80, s69, s68\n"
    "ds_read_b128 v[32:35], v8 offset:15360\n"
    "v_mfma_f32_16x16x32_bf16 v[48:51], a[52:55], v[24:27], v[48:51]\n"
    "s_cselect_b32 s86, s55, s54\n"
    "s_cselect_b32 s78, s57, s56\n"
    "ds_read_b128 v[28:31], v8 offset:14336\n"
    "v_mfma_f32_16x16x32_bf16 v[52:55], a[192:195], v[20:23], v[52:55]\n"
    "s_and_b32 s87, s80, 7\n"
    "s_mul_i32 s87, s87, s58\n"
    "s_add_i32 s86, s86, s87\n"
    "v_mfma_f32_16x16x32_bf16 v[52:55], a[196:199], v[24:27], v[52:55]\n"
    "v_mfma_f32_16x16x32_bf16 v[56:59], v[160:163], v[20:23], v[56:59]\n"
    "v_mfma_f32_16x16x32_bf16 v[56:59], v[164:167], v[24:27], v[56:59]\n"
    "s_waitcnt lgkmcnt(0)\n"
    "s_add_i32 m0, s66, 0\n"
    "s_add_i32 s74, s86, 0\n"
    "v_mfma_f32_16x16x32_bf16 v[48:51], a[56:59], v[28:31], v[48:51]\n"
    "ds_read_b128 v[24:27], v8 offset:17408\n"
    "buffer_load_dwordx4 v0, s[40:43], s74 offen offset:0 sc1 lds\n"
    "v_mfma_f32_16x16x32_bf16 v[48:51], a[60:63], v[32:35], v[48:51]\n"
    "ds_read_b128 v[20:23], v8 offset:16384\n"
    "v_mfma_f32_16x16x32_bf16 v[52:55], a[200:203], v[28:31], v[52:55]\n"
    "s_and_b32 s87, s80, 7\n"
    "s_mul_i32 s87, s87, s59\n"
    "s_add_i32 s78, s78, s87\n"
    "v_mfma_f32_16x16x32_bf16 v[52:55], a[204:207], v[32:35], v[52:55]\n"
    "s_and_b32 s92, s71, 1\n"
    "s_add_i32 s69, s69, s92\n"
    "s_lshr_b32 s92, 1, s71\n"
    "v_mfma_f32_16x16x32_bf16 v[56:59], v[168:171], v[28:31], v[56:59]\n"
    "s_add_i32 s68, s68, s92\n"
    "buffer_load_dwordx4 v0, s[40:43], s74 offen offset:1024 sc1 lds\n"
    "v_mfma_f32_16x16x32_bf16 v[56:59], v[172:175], v[32:35], v[56:59]\n"
    "s_waitcnt lgkmcnt(0)\n"
    "v_mfma_f32_16x16x32_bf16 v[48:51], a[64:67], v[20:23], v[48:51]\n"
    "ds_read_b128 v[32:35], v8 offset:19456\n"
    "v_mfma_f32_16x16x32_bf16 v[48:51], a[68:71], v[24:27], v[48:51]\n"
    "ds_read_b128 v[28:31], v8 offset:18432\n"
    "v_mfma_f32_16x16x32_bf16 v[52:55], a[208:211], v[20:23], v[52:55]\n"
    "buffer_load_dwordx4 v0, s[40:43], s74 offen offset:2048 sc1 lds\n"
    "v_mfma_f32_16x16x32_bf16 v[52:55], a[212:215], v[24:27], v[52:55]\n"
    "v_mfma_f32_16x16x32_bf16 v[56:59], v[176:179], v[20:23], v[56:59]\n"
    "v_mfma_f32_16x16x32_bf16 v[56:59], v[180:183], v[24:27], v[56:59]\n"
    "s_waitcnt lgkmcnt(0)\n"
    "v_mfma_f32_16x16x32_bf16 v[48:51], a[72:75], v[28:31], v[48:51]\n"
    "ds_read_b128 v[24:27], v8 offset:21504\n"
    "buffer_load_dwordx4 v0, s[40:43], s74 offen offset:3072 sc1 lds\n"
    "v_mfma_f32_16x16x32_bf16 v[48:51], a[76:79], v[32:35], v[48:51]\n"
    "ds_read_b128 v[20:23], v8 offset:20480\n"
    "v_mfma_f32_16x16x32_bf16 v[52:55], a[216:219], v[28:31], v[52:55]\n"
    "v_mfma_f32_16x16x32_bf16 v[52:55], a[220:223], v[32:35], v[52:55]\n"
    "v_mfma_f32_16x16x32_bf16 v[56:59], v[184:187], v[28:31], v[56:59]\n"
    "v_mfma_f32_16x16x32_bf16 v[56:59], v[188:191], v[32:35], v[56:59]\n"
    "s_waitcnt lgkmcnt(0)\n"
    "s_add_i32 m0, s67, 16384\n"
    "s_add_i32 s74, s86, 65536\n"
    "v_mfma_f32_16x16x32_bf16 v[48:51], a[80:83], v[20:23], v[48:51]\n"
    "ds_read_b128 v[32:35], v8 offset:23552\n"
    "buffer_load_dwordx4 v9, s[40:43], s74 offen offset:0 sc1 lds\n"
    "v_mfma_f32_16x16x32_bf16 v[48:51], a[84:87], v[24:27], v[48:51]\n"
    "s_waitcnt vmcnt(5)\n"
    "s_barrier\n"
    "ds_read_b128 v[28:31], v8 offset:22528\n"
    "v_mfma_f32_16x16x32_bf16 v[52:55], a[224:227], v[20:23], v[52:55]\n"
    "s_mov_b64 exec, s[90:91]\n"
    "buffer_store_dword v19, off, s[44:47], s82 sc1\n"
    "s_mov_b64 exec, -1\n"
    "v_mfma_f32_16x16x32_bf16 v[52:55], a[228:231], v[24:27], v[52:55]\n"
    "s_mov_b32 s46, s38\n"
    "s_mov_b32 s81, s75\n"
    "s_mov_b32 s82, s76\n"
    "v_mfma_f32_16x16x32_bf16 v[56:59], v[192:195], v[20:23], v[56:59]\n"
    "s_mov_b32 s83, s77\n"
    "s_mov_b32 s75, s78\n"
    "s_mov_b32 s77, s80\n"
    "v_mfma_f32_16x16x32_bf16 v[56:59], v[196:199], v[24:27], v[56:59]\n"
    "s_waitcnt lgkmcnt(0)\n"
    "s_add_i32 m0, s66, 20480\n"
    "s_add_i32 s74, s86, 32768\n"
    "v_mfma_f32_16x16x32_bf16 v[48:51], a[88:91], v[28:31], v[48:51]\n"
    "s_mul_i32 s76, s71, s94\n"
    "ds_read_b128 v[24:27], v8 offset:25600\n"
    "buffer_load_dwordx4 v0, s[40:43], s74 offen offset:0 sc1 lds\n"
    "v_mfma_f32_16x16x32_bf16 v[48:51], a[92:95], v[32:35], v[48:51]\n"
    "s_add_i32 s76, s76, s60\n"
    "s_add_i32 s72, s72, 2\n"
    "ds_read_b128 v[20:23], v8 offset:24576\n"
    "v_mfma_f32_16x16x32_bf16 v[52:55], a[232:235], v[28:31], v[52:55]\n"
    "v_mfma_f32_16x16x32_bf16 v[52:55], a[236:239], v[32:35], v[52:55]\n"
    "v_mfma_f32_16x16x32_bf16 v[56:59], v[200:203], v[28:31], v[56:59]\n"
    "v_mfma_f32_16x16x32_bf16 v[56:59], v[204:207], v[32:35], v[56:59]\n"
    "s_waitcnt lgkmcnt(0)\n"
    "v_mfma_f32_16x16x32_bf16 v[48:51], a[96:99], v[20:23], v[48:51]\n"
    "ds_read_b128 v[32:35], v8 offset:27648\n"
    "buffer_load_dwordx4 v0, s[40:43], s74 offen offset:1024 sc1 lds\n"
    "v_mfma_f32_16x16x32_bf16 v[48:51], a[100:103], v[24:27], v[48:51]\n"
    "ds_read_b128 v[28:31], v8 offset:26624\n"
    "v_mfma_f32_16x16x32_bf16 v[52:55], a[240:243], v[20:23], v[52:55]\n"
    "v_mfma_f32_16x16x32_bf16 v[52:55], a[244:247], v[24:27], v[52:55]\n"
    "v_mfma_f32_16x16x32_bf16 v[56:59], v[208:211], v[20:23], v[56:59]\n"
    "v_mfma_f32_16x16x32_bf16 v[56:59], v[212:215], v[24:27], v[56:59]\n"
    "s_waitcnt lgkmcnt(0)\n"
    "v_mfma_f32_16x16x32_bf16 v[48:51], a[104:107], v[28:31], v[48:51]\n"
    "ds_read_b128 v[24:27], v8 offset:29696\n"
    "buffer_load_dwordx4 v0, s[40:43], s74 offen offset:2048 sc1 lds\n"
    "v_mfma_f32_16x16x32_bf16 v[48:51], a[108:111], v[32:35], v[48:51]\n"
    "ds_read_b128 v[20:23], v8 offset:28672\n"
    "v_mfma_f32_16x16x32_bf16 v[52:55], a[248:251], v[28:31], v[52:55]\n"
    "v_mfma_f32_16x16x32_bf16 v[52:55], a[252:255], v[32:35], v[52:55]\n"
    "v_mfma_f32_16x16x32_bf16 v[56:59], v[216:219], v[28:31], v[56:59]\n"
    "v_mfma_f32_16x16x32_bf16 v[56:59], v[220:223], v[32:35], v[56:59]\n"
    "s_waitcnt lgkmcnt(0)\n"
    "v_mfma_f32_16x16x32_bf16 v[48:51], a[112:115], v[20:23], v[48:51]\n"
    "ds_read_b128 v[32:35], v8 offset:31744\n"
    "buffer_load_dwordx4 v0, s[40:43], s74 offen offset:3072 sc1 lds\n"
    "v_mfma_f32_16x16x32_bf16 v[48:51], a[116:119], v[24:27], v[48:51]\n"
    "ds_read_b128 v[28:31], v8 offset:30720\n"
    "v_mfma_f32_16x16x32_bf16 v[52:55], v[80:83], v[20:23], v[52:55]\n"
    "v_mfma_f32_16x16x32_bf16 v[52:55], v[84:87], v[24:27], v[52:55]\n"
    "v_mfma_f32_16x16x32_bf16 v[56:59], v[224:227], v[20:23], v[56:59]\n"
    "v_mfma_f32_16x16x32_bf16 v[56:59], v[228:231], v[24:27], v[56:59]\n"
    "s_waitcnt lgkmcnt(0)\n"
    "v_mfma_f32_16x16x32_bf16 v[48:51], a[120:123], v[28:31], v[48:51]\n"
    "ds_read_b128 v[24:27], v8 offset:33792\n"
    "v_mfma_f32_16x16x32_bf16 v[48:51], a[124:127], v[32:35], v[48:51]\n"
    "ds_read_b128 v[20:23], v8 offset:32768\n"
    "v_mfma_f32_16x16x32_bf16 v[52:55], v[88:91], v[28:31], v[52:55]\n"
    "v_mfma_f32_16x16x32_bf16 v[52:55], v[92:95], v[32:35], v[52:55]\n"
    "v_mfma_f32_16x16x32_bf16 v[56:59], v[232:235], v[28:31], v[56:59]\n"
    "v_mfma_f32_16x16x32_bf16 v[56:59], v[236:239], v[32:35], v[56:59]\n"
    "s_waitcnt lgkmcnt(0)\n"
    "v_mfma_f32_16x16x32_bf16 v[48:51], a[128:131], v[20:23], v[48:51]\n"
    "ds_read_b128 v[32:35], v8 offset:35840\n"
    "global_load_dword v16, v3, s[52:53] sc1\n"
    "v_mfma_f32_16x16x32_bf16 v[48:51], a[132:135], v[24:27], v[48:51]\n"
    "ds_read_b128 v[28:31], v8 offset:34816\n"
    "v_mfma_f32_16x16x32_bf16 v[52:55], v[96:99], v[20:23], v[52:55]\n"
    "v_mfma_f32_16x16x32_bf16 v[52:55], v[100:103], v[24:27], v[52:55]\n"
    "v_mfma_f32_16x16x32_bf16 v[56:59], v[240:243], v[20:23], v[56:59]\n"
    "v_mfma_f32_16x16x32_bf16 v[56:59], v[244:247], v[24:27], v[56:59]\n"
    "s_waitcnt lgkmcnt(0)\n"
    "v_mfma_f32_16x16x32_bf16 v[48:51], a[136:139], v[28:31], v[48:51]\n"
    "v_mfma_f32_16x16x32_bf16 v[48:51], a[140:143], v[32:35], v[48:51]\n"
    "s_waitcnt vmcnt(6) lgkmcnt(0)\n"
    "s_barrier\n"
    "ds_read_b128 v[24:27], v1 offset:1024\n"
    "v_mfma_f32_16x16x32_bf16 v[52:55], v[104:107], v[28:31], v[52:55]\n"
    "ds_read_b128 v[20:23], v1 offset:0\n"
    "v_mfma_f32_16x16x32_bf16 v[52:55], v[108:111], v[32:35], v[52:55]\n"
    "v_mfma_f32_16x16x32_bf16 v[56:59], v[248:251], v[28:31], v[56:59]\n"
    "v_mfma_f32_16x16x32_bf16 v[56:59], v[252:255], v[32:35], v[56:59]\n"
    "s_cmp_eq_u32 s42, 0\n"
    "s_cbranch_scc0 L_body0%=\n"
    "L_idle0%=:\n"
    "s_nop 4\n"
    "v_mov_b32 v19, s83\n"
    "v_cvt_pk_bf16_f32 v60, v48, v49\n"
    "v_cvt_pk_bf16_f32 v61, v50, v51\n"
    "v_cvt_pk_bf16_f32 v62, v52, v53\n"
    "v_cvt_pk_bf16_f32 v63, v54, v55\n"
    "v_cvt_pk_bf16_f32 v64, v56, v57\n"
    "v_cvt_pk_bf16_f32 v65, v58, v59\n"
    "ds_write_b64 v6, v[60:61] offset:0\n"
    "ds_write_b64 v6, v[62:63] offset:32\n"
    "ds_write_b64 v6, v[64:65] offset:64\n"
    "ds_read_b128 v[60:63], v7 offset:0\n"
    "ds_read_b128 v[64:67], v7 offset:896\n"
    "s_waitcnt lgkmcnt(0)\n"
    "buffer_store_dwordx4 v[60:63], v4, s[44:47], s81 offen sc1\n"
    "s_add_i32 s73, s81, s62\n"
    "buffer_store_dwordx4 v[64:67], v4, s[44:47], s73 offen sc1\n"
    "s_waitcnt vmcnt(2)\n"
    "v_add_u32 v16, v16, v11\n"
    "v_cmp_gt_i32 s[92:93], s68, v16\n"
    "v_cmp_gt_i32 vcc, s69, v16\n"
    "s_cmp_eq_u32 vcc_hi, 0\n"
    "s_cselect_b32 s93, 1, 2\n"
    "s_cmp_eq_u32 s92, 0\n"
    "s_cselect_b32 s87, 0, s93\n"
    "v_mov_b32 v17, s87\n"
    "ds_write_b32 v12, v17 offset:16\n"
    "s_waitcnt lgkmcnt(0)\n"
    "s_barrier\n"
    "ds_read_b32 v16, v10 offset:16\n"
    "s_waitcnt lgkmcnt(0)\n"
    "v_readfirstlane_b32 s71, v16\n"
    "s_cmp_lg_u32 s71, 2\n"
    "s_cbranch_scc1 L_go0%=\n"
    "s_waitcnt vmcnt(0)\n"
    "s_barrier\n"
    "s_mov_b64 exec, s[90:91]\n"
    "buffer_store_dword v19, off, s[44:47], s82 sc1\n"
    "s_mov_b64 exec, -1\n"
    "s_mov_b32 s46, 0\n"
    "s_cmp_ge_u32 s72, 128\n"
    "s_cbranch_scc1 L_exit%=\n"
    "L_start%=:\n"
    "s_cmp_lg_u32 s65, 0\n"
    "s_cbranch_scc1 L_got0%=\n"
    "L_poll0%=:\n"
    "global_load_dword v16, v3, s[52:53] sc1\n"
    "s_waitcnt vmcnt(0)\n"
    "v_add_u32 v16, v16, v11\n"
    "v_cmp_gt_i32 s[92:93], s68, v16\n"
    "v_cmp_gt_i32 vcc, s69, v16\n"
    "s_cmp_eq_u32 vcc_hi, 0\n"
    "s_cselect_b32 s93, 1, 2\n"
    "s_cmp_eq_u32 s92, 0\n"
    "s_cselect_b32 s87, 0, s93\n"
    "s_cmp_lg_u32 s87, 2\n"
    "s_cbranch_scc1 L_rdy0%=\n"
    "s_memrealtime s[88:89]\n"
    "s_waitcnt lgkmcnt(0)\n"
    "s_sub_i32 s74, s64, s88\n"
    "s_cmp_ge_u32 s68, 64\n"
    "s_cselect_b32 s87, 1, 0\n"
    "s_cmp_lt_i32 s74, 0\n"
    "s_cbranch_scc1 L_rdy0%=\n"
    "s_sleep 2\n"
    "s_branch L_poll0%=\n"
    "L_rdy0%=:\n"
    "v_mov_b32 v17, s87\n"
    "ds_write_b32 v10, v17 offset:32\n"
    "s_waitcnt lgkmcnt(0)\n"
    "L_got0%=:\n"
    "s_barrier\n"
    "ds_read_b32 v16, v10 offset:32\n"
    "s_waitcnt lgkmcnt(0)\n"
    "v_readfirstlane_b32 s71, v16\n"
    "L_go0%=:\n"
    "s_cmp_eq_u32 s71, 1\n"
    "s_cselect_b32 s80, s69, s68\n"
    "s_cselect_b32 s86, s55, s54\n"
    "s_cselect_b32 s75, s57, s56\n"
    "s_and_b32 s87, s80, 7\n"
    "s_mul_i32 s87, s87, s58\n"
    "s_add_i32 s86, s86, s87\n"
    "s_add_i32 m0, s66, 0\n"
    "s_add_i32 s74, s86, 0\n"
    "buffer_load_dwordx4 v0, s[36:39], s74 offen offset:0 sc1 lds\n"
    "buffer_load_dwordx4 v0, s[36:39], s74 offen offset:1024 sc1 lds\n"
    "buffer_load_dwordx4 v0, s[36:39], s74 offen offset:2048 sc1 lds\n"
    "buffer_load_dwordx4 v0, s[36:39], s74 offen offset:3072 sc1 lds\n"
    "s_add_i32 m0, s67, 16384\n"
    "s_add_i32 s74, s86, 65536\n"
    "buffer_load_dwordx4 v9, s[36:39], s74 offen offset:0 sc1 lds\n"
    "s_add_i32 m0, s66, 20480\n"
    "s_add_i32 s74, s86, 32768\n"
    "buffer_load_dwordx4 v0, s[36:39], s74 offen offset:0 sc1 lds\n"
    "buffer_load_dwordx4 v0, s[36:39], s74 offen offset:1024 sc1 lds\n"
    "buffer_load_dwordx4 v0, s[36:39], s74 offen offset:2048 sc1 lds\n"
    "buffer_load_dwordx4 v0, s[36:39], s74 offen offset:3072 sc1 lds\n"
    "s_and_b32 s87, s80, 7\n"
    "s_mul_i32 s87, s87, s59\n"
    "s_add_i32 s75, s75, s87\n"
    "s_and_b32 s92, s71, 1\n"
    "s_add_i32 s69, s69, s92\n"
    "s_lshr_b32 s92, 1, s71\n"
    "s_add_i32 s68, s68, s92\n"
    "s_mov_b32 s77, s80\n"
    "s_mul_i32 s76, s71, s94\n"
    "s_add_i32 s76, s76, s60\n"
    "s_waitcnt vmcnt(5)\n"
    "s_barrier\n"
    "ds_read_b128 v[24:27], v1 offset:1024\n"
    "ds_read_b128 v[20:23], v1 offset:0\n"
    "global_load_dword v16, v3, s[52:53] sc1\n"
    "s_mov_b64 exec, s[90:91]\n"
    "buffer_store_dword v19, off, s[44:47], s82 sc1\n"
    "s_mov_b64 exec, -1\n"
    "s_mov_b32 s46, 0\n"
    "s_branch L_body0%=\n"
    "L_idle1%=:\n"
    "s_nop 4\n"
    "v_mov_b32 v19, s83\n"
    "v_cvt_pk_bf16_f32 v60, v36, v37\n"
    "v_cvt_pk_bf16_f32 v61, v38, v39\n"
    "v_cvt_pk_bf16_f32 v62, v40, v41\n"
    "v_cvt_pk_bf16_f32 v63, v42, v43\n"
    "v_cvt_pk_bf16_f32 v64, v44, v45\n"
    "v_cvt_pk_bf16_f32 v65, v46, v47\n"
    "ds_write_b64 v6, v[60:61] offset:0\n"
    "ds_write_b64 v6, v[62:63] offset:32\n"
    "ds_write_b64 v6, v[64:65] offset:64\n"
    "ds_read_b128 v[60:63], v7 offset:0\n"
    "ds_read_b128 v[64:67], v7 offset:896\n"
    "s_waitcnt lgkmcnt(0)\n"
    "buffer_store_dwordx4 v[60:63], v4, s[44:47], s81 offen sc1\n"
    "s_add_i32 s73, s81, s62\n"
    "buffer_store_dwordx4 v[64:67], v4, s[44:47], s73 offen sc1\n"
    "s_waitcnt vmcnt(2)\n"
    "v_add_u32 v16, v16, v11\n"
    "v_cmp_gt_i32 s[92:93], s68, v16\n"
    "v_cmp_gt_i32 vcc, s69, v16\n"
    "s_cmp_eq_u32 vcc_hi, 0\n"
    "s_cselect_b32 s93, 1, 2\n"
    "s_cmp_eq_u32 s92, 0\n"
    "s_cselect_b32 s87, 0, s93\n"
    "v_mov_b32 v17, s87\n"
    "ds_write_b32 v12, v17 offset:16\n"
    "s_waitcnt lgkmcnt(0)\n"
    "s_barrier\n"
    "ds_read_b32 v16, v10 offset:16\n"
    "s_waitcnt lgkmcnt(0)\n"
    "v_readfirstlane_b32 s71, v16\n"
    "s_cmp_lg_u32 s71, 2\n"
    "s_cbranch_scc1 L_go1%=\n"
    "s_waitcnt vmcnt(0)\n"
    "s_barrier\n"
    "s_mov_b64 exec, s[90:91]\n"
    "buffer_store_dword v19, off, s[44:47], s82 sc1\n"
    "s_mov_b64 exec, -1\n"
    "s_mov_b32 s46, 0\n"
    "s_cmp_lg_u32 s65, 0\n"
    "s_cbranch_scc1 L_got1%=\n"
    "L_poll1%=:\n"
    "global_load_dword v16, v3, s[52:53] sc1\n"
    "s_waitcnt vmcnt(0)\n"
    "v_add_u32 v16, v16, v11\n"
    "v_cmp_gt_i32 s[92:93], s68, v16\n"
    "v_cmp_gt_i32 vcc, s69, v16\n"
    "s_cmp_eq_u32 vcc_hi, 0\n"
    "s_cselect_b32 s93, 1, 2\n"
    "s_cmp_eq_u32 s92, 0\n"
    "s_cselect_b32 s87, 0, s93\n"
    "s_cmp_lg_u32 s87, 2\n"
    "s_cbranch_scc1 L_rdy1%=\n"
    "s_memrealtime s[88:89]\n"
    "s_waitcnt lgkmcnt(0)\n"
    "s_sub_i32 s74, s64, s88\n"
    "s_cmp_ge_u32 s68, 64\n"
    "s_cselect_b32 s87, 1, 0\n"
    "s_cmp_lt_i32 s74, 0\n"
    "s_cbranch_scc1 L_rdy1%=\n"
    "s_sleep 2\n"
    "s_branch L_poll1%=\n"
    "L_rdy1%=:\n"
    "v_mov_b32 v17, s87\n"
    "ds_write_b32 v10, v17 offset:32\n"
    "s_waitcnt lgkmcnt(0)\n"
    "L_got1%=:\n"
    "s_barrier\n"
    "ds_read_b32 v16, v10 offset:32\n"
    "s_waitcnt lgkmcnt(0)\n"
    "v_readfirstlane_b32 s71, v16\n"
    "L_go1%=:\n"
    "s_cmp_eq_u32 s71, 1\n"
    "s_cselect_b32 s80, s69, s68\n"
    "s_cselect_b32 s86, s55, s54\n"
    "s_cselect_b32 s75, s57, s56\n"
    "s_and_b32 s87, s80, 7\n"
    "s_mul_i32 s87, s87, s58\n"
    "s_add_i32 s86, s86, s87\n"
    "s_add_i32 m0, s66, 36864\n"
    "s_add_i32 s74, s86, 0\n"
    "buffer_load_dwordx4 v0, s[36:39], s74 offen offset:0 sc1 lds\n"
    "buffer_load_dwordx4 v0, s[36:39], s74 offen offset:1024 sc1 lds\n"
    "buffer_load_dwordx4 v0, s[36:39], s74 offen offset:2048 sc1 lds\n"
    "buffer_load_dwordx4 v0, s[36:39], s74 offen offset:3072 sc1 lds\n"
    "s_add_i32 m0, s67, 53248\n"
    "s_add_i32 s74, s86, 65536\n"
    "buffer_load_dwordx4 v9, s[36:39], s74 offen offset:0 sc1 lds\n"
    "s_add_i32 m0, s66, 57344\n"
    "s_add_i32 s74, s86, 32768\n"
    "buffer_load_dwordx4 v0, s[36:39], s74 offen offset:0 sc1 lds\n"
    "buffer_load_dwordx4 v0, s[36:39], s74 offen offset:1024 sc1 lds\n"
    "buffer_load_dwordx4 v0, s[36:39], s74 offen offset:2048 sc1 lds\n"
    "buffer_load_dwordx4 v0, s[36:39], s74 offen offset:3072 sc1 lds\n"
    "s_and_b32 s87, s80, 7\n"
    "s_mul_i32 s87, s87, s59\n"
    "s_add_i32 s75, s75, s87\n"
    "s_and_b32 s92, s71, 1\n"
    "s_add_i32 s69, s69, s92\n"
    "s_lshr_b32 s92, 1, s71\n"
    "s_add_i32 s68, s68, s92\n"
    "s_mov_b32 s77, s80\n"
    "s_mul_i32 s76, s71, s94\n"
    "s_add_i32 s76, s76, s60\n"
    "s_waitcnt vmcnt(5)\n"
    "s_barrier\n"
    "ds_read_b128 v[24:27], v8 offset:1024\n"
    "ds_read_b128 v[20:23], v8 offset:0\n"
    "global_load_dword v16, v3, s[52:53] sc1\n"
    "s_mov_b64 exec, s[90:91]\n"
    "buffer_store_dword v19, off, s[44:47], s82 sc1\n"
    "s_mov_b64 exec, -1\n"
    "s_mov_b32 s46, 0\n"
    "s_branch L_body1%=\n"
    "L_exit%=:\n"
    : GEMM_OUT : GEMM_IN, GEMM_IN3, "{v[208:239]}"(W.a[12]), "{v[240:255]}"(W.b) : PART_CLOBBERS);
  __builtin_amdgcn_endpgm();
}

DI void qkv_role(const Params& p, Ctx& c, LAS char* L, int j) {
  const int lane = c.lane, w = c.w, h = j >> 1, a = j & 1;
  GemmW W;
  {
    rsrc_t rp = mk(w < 2 ? p.wq : p.wk, 576u * 8192u), rv = mk(p.wv, 576u * 8192u);
    unsigned v1 = (unsigned)(piece_k(lane) * 8192 + (lane & 1) * 16), v2 = (unsigned)((lane >> 2) * 8192) + pair_chunk(lane);
    unsigned cp = (unsigned)(h * 128 + 32 * (2 * a + (w & 1))) * 2u, cv = (unsigned)(h * 128 + 16 * (4 * a + w)) * 2u;
    load_frags<54, 18>(L + w * 40960, lane, [](auto ic) { return IC<decltype(ic)::v % 3>{}; },
      [&](auto ic, LAS char* d) { constexpr int i = decltype(ic)::v; constexpr int k = i / 3, t = i % 3;
        if constexpr (t < 2) dmaw(rp, v2, cp + (k * 32 + t * 16) * 8192, d); else dmaw(rv, v1, cv + k * 32 * 8192, d); },
      [&](auto ic, v4u f) { constexpr int i = decltype(ic)::v; putw<(i % 3) * 18 + i / 3, 54>(W, f); });
  }
  get_par(c);
  BAR();
  GemmCfg g;
  g.in0 = 0; g.in1 = 4 * PG; g.in_stride = 16384;
  unsigned po = (unsigned)((w < 2 ? 0 : 16384) + (2 * a + (w & 1)) * 2048);
  g.out0 = head_base(c, 0, h) + po; g.out1 = head_base(c, 1, h) + po; g.out_stride = 49152;
  g.node = N_QKV + j; g.alt = 1;
  g.flagv = (lane & 16) ? flagoff(c, N_AT + (lane >> 5) * 8 + (h >> 2), 0) : (lane < 32 ? flagoff(c, N_RD + (lane & 15), 1) : flagoff(c, N_RD + 4 + (lane & 3), 0));
  g.flaga = (lane & 16) ? RQ : 0u;
  g.ov0 = lane * 16; g.ov1 = 0; g.os1 = 1024; g.os2 = 32768u + (unsigned)(4 * a + w) * 1024u - po;
  gemm_loop<M_QKV>(c, g, L, W);
}

DI void gu_role(const Params& p, Ctx& c, LAS char* L, int j) {
  const int lane = c.lane, w = c.w;
  GemmW W;
  nops(TV(2, DLY_GU));
  {
    unsigned cb = (unsigned)(96 * j + 24 * w) * 2u;
    unsigned ch = pair_chunk(lane);
    const char* p2 = ((ch & 32) ? p.wu : p.wg) + (lane >> 2) * 9216 + cb + (ch & 16);
    const char* p1 = ((lane & 1) ? p.wu : p.wg) + piece_k(lane) * 9216 + cb + 32;
    load_frags<54, 18>(L + w * 40960, lane, [](auto ic) { return IC<decltype(ic)::v % 3>{}; },
      [&](auto ic, LAS char* d) { constexpr int i = decltype(ic)::v; constexpr int k = i / 3, t = i % 3;
        __builtin_amdgcn_global_load_lds((const GAS void*)(t < 2 ? p2 + (k * 32 + t * 16) * 9216 : p1 + k * 32 * 9216), (LAS void*)d, 16, 0, 0); },
      [&](auto ic, v4u f) { constexpr int i = decltype(ic)::v; putw<(i % 3) * 18 + i / 3, 54>(W, f); });
  }
  get_par(c);
  BAR();
  GemmCfg g;
  g.in0 = 0; g.in1 = 4 * PG; g.in_stride = 16384;
  g.out0 = near(c, OFF_HB + (unsigned)(j / 12) * PG, HDIE) + (unsigned)w * 256u; g.out1 = g.out0 + LSTR; g.out_stride = 81920;
  g.node = N_GU + j; g.alt = 0;
  g.flagv = (lane & 16) ? flagoff(c, N_DN + (j / 12) * 6 + (lane & 15) % 6, lane >> 5) : flagoff(c, N_RD + (lane >> 5) * 8 + (lane & 3), 0);
  g.flaga = (lane & 16) ? RB : 0u;
  { int tok = lane & 15, gg = lane >> 4;
    auto pos = [](int k) { int q = k < 16 ? k : (k < 20 ? k + 16 : k - 4); return (q >> 3) * 16384 + (q & 7) * 1024; };
    g.ov0 = (unsigned)((gg & 1) * 1048576 + pos(3 * (j % 12) + (gg >> 1)) + tok * 16);
    g.ov1 = (gg & 1) ? OOB : (unsigned)((gg >> 1) * 1048576 + pos(3 * (j % 12) + 2) + tok * 16); }
  g.os1 = 0; g.os2 = 0;
  gemm_loop<M_GU>(c, g, L, W);
}

template <int KS> DI void part_role(const Params& p, Ctx& c, LAS char* L, int idx) {
  constexpr bool WO = KS == 32;
  const int lane = c.lane, w = c.w, kq = idx / 6, m = (idx / 3) & 1, ns = idx % 3;
  GemmW W;
  nops(WO ? TV(3, DLY_WO) : TV(4, DLY_DN));
  {
    rsrc_t rw = mk(WO ? p.wo : p.wd, (WO ? 4096u : 4608u) * 1152u);
    auto rowf = [&](int kk) { int gg = kk >> 3, j8 = kk & 7; return WO ? (j8 < 4 ? 4 * gg + j8 : 12 + 4 * gg + j8) : (24 * gg + j8); };
    unsigned v1 = (unsigned)(rowf(piece_k(lane)) * 1152 + (lane & 1) * 16), v2 = (unsigned)(rowf(lane >> 2) * 1152) + pair_chunk(lane);
    unsigned cp = (unsigned)(kq * KS * 32) * 1152u + (unsigned)(192 * ns + 48 * w) * 2u;
    load_frags<3 * KS, WO ? 12 : 18>(L + w * 40960, lane, [](auto ic) { return IC<decltype(ic)::v % 3>{}; },
      [&](auto ic, LAS char* d) { constexpr int i = decltype(ic)::v; constexpr int k = i / 3, t = i % 3;
        constexpr unsigned ro = (WO ? k * 32 : (k / 3) * 96 + (k % 3) * 8) * 1152u;
        if constexpr (t < 2) dmaw(rw, v2, cp + ro + t * (WO ? 8 : 48) * 1152, d); else dmaw(rw, v1, cp + 64 + ro, d); },
      [&](auto ic, v4u f) { constexpr int i = decltype(ic)::v; putw<(i % 3) * KS + i / 3, 3 * KS>(W, f); });
  }
  get_par(c);
  BAR();
  GemmCfg g;
  g.in0 = WO ? near(c, OFF_O + (unsigned)(kq * 2 + m) * 524288u, ODIE) : near(c, OFF_HB + (unsigned)(kq * 2 + m) * 1048576u, HDIE); g.in1 = g.in0 + LSTR; g.in_stride = WO ? 65536u : 81920u;
  g.out0 = near(c, (WO ? OFF_PW : OFF_PD) + (unsigned)m * PG, 1u) + (unsigned)(kq * 65536 + (4 * ns + w) * 384); g.out1 = g.out0 + LSTR; g.out_stride = 262144;
  g.node = (WO ? N_WO : N_DN) + idx; g.alt = 0;
  { int l = lane >> 5, q = lane & 15;
    g.flagv = (lane & 16) ? flagoff(c, N_RD + l * 8 + (WO ? 0 : 4) + m * 2 + (q & 1), 0) : (WO ? flagoff(c, N_AT + l * 8 + kq * 2 + (q & 1), 0) : flagoff(c, N_GU + kq * 12 + q % 12, l));
    g.flaga = (lane & 16) ? RB : 0u; }
  g.ov0 = lane < 48 ? (unsigned)((lane / 24) * 19968 + (lane % 24) * 16) : OOB; g.ov1 = 0;
  g.os1 = 32768; g.os2 = 0;
  gemm_loop<WO ? M_WO : M_DN>(c, g, L, W);
}

DI void ln_row(const v2u (&xi)[5], bool tail_ok, v2u (&o)[5]) {
  float x[20];
  #pragma unroll
  for (int i = 0; i < 5; ++i) { x[4*i] = blo(xi[i].x); x[4*i+1] = bhi(xi[i].x); x[4*i+2] = blo(xi[i].y); x[4*i+3] = bhi(xi[i].y); }
  float s = 0;
  #pragma unroll
  for (int i = 0; i < 20; ++i) s += x[i];
  float mean = rsum(s) * (1.0f / 576.0f);
  float q = 0, qt = 0;
  #pragma unroll
  for (int i = 0; i < 16; ++i) { float d = x[i] - mean; q = __builtin_fmaf(d, d, q); }
  #pragma unroll
  for (int i = 16; i < 20; ++i) { float d = x[i] - mean; qt = __builtin_fmaf(d, d, qt); }
  float var = rsum(q + (tail_ok ? qt : 0.0f)) * (1.0f / 576.0f);
  float inv = __builtin_amdgcn_rsqf(var + 1e-5f);
  #pragma unroll
  for (int i = 0; i < 5; ++i) o[i] = (v2u){pk((x[4*i] - mean) * inv, (x[4*i+1] - mean) * inv), pk((x[4*i+2] - mean) * inv, (x[4*i+3] - mean) * inv)};
}

DI void red_role(const Params& p, Ctx& c, int idx) {
  const int lane = c.lane, st = idx >> 2, a = (idx & 3) * 4 + c.w, node = N_RD + idx;
  const int g = lane >> 4, cc = lane & 15;
  const bool tail_ok = g < 2;
  const unsigned xv = (unsigned)((a + 16 * (g & 1)) * 1152 + (g >> 1) * 128 + cc * 8), xvt = tail_ok ? xv : OOB;
  rsrc_t rx = mk(p.x, XBYTES);
  {
    unsigned xa = 0, xat = 0;
    auto seg = [&](int q0, auto nc) { constexpr int N = decltype(nc)::v;
      v2u in[N][5];
      #pragma unroll
      for (int q = 0; q < N; ++q)
        #pragma unroll
        for (int i = 0; i < 5; ++i) in[q][i] = __builtin_amdgcn_raw_buffer_load_b64(rx, (i < 4 ? xv : xvt) + i * 256, (unsigned)((st + 4 * (q0 + q)) * 36864), 0);
      if (q0 == 0) { get_par(c); xa = xrow(c, X_A0, a + 16 * (g & 1)) + (unsigned)((g >> 1) * 128 + cc * 8); xat = tail_ok ? xa : OOB; }
      #pragma unroll
      for (int q = 0; q < N; ++q) {
        v2u o[5];
        ln_row(in[q], tail_ok, o);
        #pragma unroll
        for (int i = 0; i < 5; ++i) st2(o[i], c.ws, (i < 4 ? xa : xat) + i * 256, (unsigned)((st + 4 * (q0 + q)) * 16384));
      }
      WAITV(0); BAR(); publish(c, node, 1, 4 * (q0 + N) - 1); STAMPT(node, 64 + q0 + N - 1); };
    seg(0, IC<1>{});
    for (int q0 = 1; q0 < 16; q0 += 3) seg(q0, IC<3>{});
  }
  const int l = st >> 1, dn = st & 1;
  auto desc = [](v16u& d, int o, const char* q, unsigned n) { unsigned long long a = (unsigned long long)q; d[o] = (unsigned)a; d[o + 1] = (unsigned)(a >> 32) & 0xFFFFu; d[o + 2] = n; d[o + 3] = 0x00020000u; };
  v16u s0; v8u s1, c0; v4u sel0, sel1, c1;
  desc(s0, 0, c.wsp, WS_USED);
  desc(s0, 4, st == 0 ? p.x : c.wsp, st == 0 ? XBYTES : WS_USED);
  desc(s0, 8, st == 3 ? p.out : c.wsp, st == 3 ? XBYTES : WS_USED);
  s0[12] = st == 0 ? 36864u : 16384u; s0[13] = st == 3 ? 36864u : 16384u; s0[14] = (unsigned)(3 - st); s0[15] = 0;
  const int m = (idx >> 1) & 1;
  s1[0] = (unsigned)(unsigned long long)c.wsp; s1[1] = (unsigned)((unsigned long long)c.wsp >> 32); s1[2] = near(c, (dn ? OFF_PD : OFF_PW) + (unsigned)l * LSTR + (unsigned)m * PG, 1u);
  s1[3] = flagoff(c, node, 0); s1[4] = (unsigned)c.deadline; s1[5] = (unsigned)c.w; s1[6] = (unsigned)node; s1[7] = 0;
  { int t = 8 * (idx & 1) + 2 * c.w + (cc >> 3), c7 = cc & 7, row = 16 * m + t;
    unsigned col = (unsigned)(((g & 1) * 2 + (g >> 1)) * 128 + c7 * 16);
    c0[0] = (st == 0 ? (unsigned)row * 1152u : xrow(c, X_R0 + st - 1, row)) + col;
    c0[2] = (st == 3 ? (unsigned)row * 1152u : xrow(c, X_R0 + st, row)) + col;
    c0[4] = xrow(c, st == 0 ? X_B0 : (st == 1 ? X_A1 : X_B1), row) + col;
    c0[1] = g == 0 ? c0[0] : OOB; c0[3] = g == 0 ? c0[2] : OOB; c0[5] = g == 0 ? c0[4] : OOB; c0[6] = 0; c0[7] = 0;
    #pragma unroll
    for (int u = 0; u < 3; ++u) { int ch = 8 * u + c7; c1[u] = (unsigned)(g * 65536 + (t >> 2) * 16384 + ((t >> 2) & 1) * 3584 + (ch / 6) * 384 + (t & 3) * 96 + (ch % 6) * 16); }
    int q = lane < 12 ? lane : 11;
    c1[3] = flagoff(c, (dn ? N_DN : N_WO) + (q / 3) * 6 + m * 3 + q % 3, l); }
  { unsigned one = 0x3F80u << (16 * (cc & 1)); int d = cc >> 1;
    sel0 = (v4u){d == 0 ? one : 0u, d == 1 ? one : 0u, d == 2 ? one : 0u, d == 3 ? one : 0u};
    sel1 = (v4u){d == 4 ? one : 0u, d == 5 ? one : 0u, d == 6 ? one : 0u, d == 7 ? one : 0u}; }
  uni<16>(s0); uni<8>(s1);
  asm volatile(
    "s_nop 4\n"
    "s_lshr_b32 s66, 2, s57\n"
    "s_and_b32 s66, s66, 1\n"
    "s_mov_b32 s67, 0\n"
    "s_mov_b32 s68, -1\n"
    "s_mov_b32 s69, 0\n"
    "s_mov_b32 s60, 0\n"
    "s_mov_b32 s61, 0\n"
    "s_mov_b32 s70, 0\n"
    "s_mov_b32 s71, 0\n"
    "L_job%=:\n"
    "s_and_b32 s63, s60, 7\n"
    "s_lshl_b32 s63, s63, 18\n"
    "s_add_i32 s63, s63, s54\n"
    "s_cmp_lg_u32 s57, 0\n"
    "s_cbranch_scc1 L_got%=\n"
    "L_poll%=:\n"
    "global_load_dword v20, v87, s[52:53] sc1\n"
    "s_waitcnt vmcnt(0)\n"
    "v_cmp_gt_i32 vcc, s60, v20\n"
    "s_cbranch_vccz L_got%=\n"
    "s_memrealtime s[64:65]\n"
    "s_waitcnt lgkmcnt(0)\n"
    "s_sub_i32 s62, s56, s64\n"
    "s_cmp_lt_i32 s62, 0\n"
    "s_cbranch_scc1 L_got%=\n"
    "s_sleep 2\n"
    "s_branch L_poll%=\n"
    "L_got%=:\n"
    "s_barrier\n"
    "buffer_load_dwordx4 v[48:51], v0, s[40:43], s70 offen offset:0 sc1\n"
    "buffer_load_dwordx4 v[52:55], v0, s[40:43], s70 offen offset:512 sc1\n"
    "buffer_load_dwordx4 v[56:59], v1, s[40:43], s70 offen offset:1024 sc1\n"
    "buffer_load_dwordx4 a[0:3], v84, s[36:39], s63 offen offset:0 sc1\n"
    "buffer_load_dwordx4 a[4:7], v85, s[36:39], s63 offen offset:0 sc1\n"
    "buffer_load_dwordx4 a[8:11], v86, s[36:39], s63 offen offset:0 sc1\n"
    "buffer_load_dwordx4 a[12:15], v84, s[36:39], s63 offen offset:1536 sc1\n"
    "buffer_load_dwordx4 a[16:19], v85, s[36:39], s63 offen offset:1536 sc1\n"
    "buffer_load_dwordx4 a[20:23], v86, s[36:39], s63 offen offset:1536 sc1\n"
    "buffer_load_dwordx4 a[24:27], v84, s[36:39], s63 offen offset:3072 sc1\n"
    "buffer_load_dwordx4 a[28:31], v85, s[36:39], s63 offen offset:3072 sc1\n"
    "buffer_load_dwordx4 a[32:35], v86, s[36:39], s63 offen offset:3072 sc1\n"
    "s_waitcnt vmcnt(9)\n"
    "v_permlane16_swap_b32 v48, v50\n"
    "v_permlane16_swap_b32 v49, v51\n"
    "v_permlane16_swap_b32 v52, v54\n"
    "v_permlane16_swap_b32 v53, v55\n"
    "v_permlane16_swap_b32 v56, v58\n"
    "v_permlane16_swap_b32 v57, v59\n"
    "v_lshlrev_b32 v28, 16, v48\n"
    "v_and_b32 v29, 0xffff0000, v48\n"
    "v_lshlrev_b32 v30, 16, v49\n"
    "v_and_b32 v31, 0xffff0000, v49\n"
    "v_lshlrev_b32 v32, 16, v50\n"
    "v_and_b32 v33, 0xffff0000, v50\n"
    "v_lshlrev_b32 v34, 16, v51\n"
    "v_and_b32 v35, 0xffff0000, v51\n"
    "v_lshlrev_b32 v36, 16, v52\n"
    "v_and_b32 v37, 0xffff0000, v52\n"
    "v_lshlrev_b32 v38, 16, v53\n"
    "v_and_b32 v39, 0xffff0000, v53\n"
    "v_lshlrev_b32 v40, 16, v54\n"
    "v_and_b32 v41, 0xffff0000, v54\n"
    "v_lshlrev_b32 v42, 16, v55\n"
    "v_and_b32 v43, 0xffff0000, v55\n"
    "v_lshlrev_b32 v44, 16, v56\n"
    "v_and_b32 v45, 0xffff0000, v56\n"
    "v_lshlrev_b32 v46, 16, v57\n"
    "v_and_b32 v47, 0xffff0000, v57\n"
    "s_waitcnt vmcnt(8)\n"
    "v_mfma_f32_16x16x32_bf16 v[28:31], v[8:11], a[0:3], v[28:31]\n"
    "s_waitcnt vmcnt(7)\n"
    "v_mfma_f32_16x16x32_bf16 v[28:31], v[16:19], a[4:7], v[28:31]\n"
    "s_waitcnt vmcnt(6)\n"
    "v_mfma_f32_16x16x32_bf16 v[32:35], v[8:11], a[8:11], v[32:35]\n"
    "s_waitcnt vmcnt(5)\n"
    "v_mfma_f32_16x16x32_bf16 v[32:35], v[16:19], a[12:15], v[32:35]\n"
    "s_waitcnt vmcnt(4)\n"
    "v_mfma_f32_16x16x32_bf16 v[36:39], v[8:11], a[16:19], v[36:39]\n"
    "s_waitcnt vmcnt(3)\n"
    "v_mfma_f32_16x16x32_bf16 v[36:39], v[16:19], a[20:23], v[36:39]\n"
    "s_waitcnt vmcnt(2)\n"
    "v_mfma_f32_16x16x32_bf16 v[40:43], v[8:11], a[24:27], v[40:43]\n"
    "s_waitcnt vmcnt(1)\n"
    "v_mfma_f32_16x16x32_bf16 v[40:43], v[16:19], a[28:31], v[40:43]\n"
    "s_waitcnt vmcnt(0)\n"
    "v_mfma_f32_16x16x32_bf16 v[44:47], v[8:11], a[32:35], v[44:47]\n"
    "v_pk_add_f32 v[22:23], v[28:29], v[30:31]\n"
    "v_pk_add_f32 v[24:25], v[32:33], v[34:35]\n"
    "v_pk_add_f32 v[22:23], v[22:23], v[36:37]\n"
    "v_pk_add_f32 v[24:25], v[24:25], v[38:39]\n"
    "s_cmp_eq_u32 s50, 0\n"
    "v_pk_add_f32 v[22:23], v[22:23], v[40:41]\n"
    "v_pk_add_f32 v[24:25], v[24:25], v[42:43]\n"
    "s_nop 1\n"
    "v_pk_add_f32 v[22:23], v[22:23], v[44:45]\n"
    "v_pk_add_f32 v[24:25], v[24:25], v[46:47]\n"
    "v_pk_add_f32 v[22:23], v[22:23], v[24:25]\n"
    "s_cbranch_scc1 L_last%=\n"
    "v_add_f32 v22, v22, v23\n"
    "v_cvt_pk_bf16_f32 v60, v28, v29\n"
    "v_cvt_pk_bf16_f32 v61, v30, v31\n"
    "v_add_f32_dpp v22, v22, v22 quad_perm:[1,0,3,2] row_mask:0xf bank_mask:0xf\n"
    "v_cvt_pk_bf16_f32 v62, v32, v33\n"
    "v_cvt_pk_bf16_f32 v63, v34, v35\n"
    "v_add_f32_dpp v22, v22, v22 quad_perm:[2,3,0,1] row_mask:0xf bank_mask:0xf\n"
    "v_cvt_pk_bf16_f32 v64, v36, v37\n"
    "v_cvt_pk_bf16_f32 v65, v38, v39\n"
    "v_add_f32_dpp v22, v22, v22 row_half_mirror row_mask:0xf bank_mask:0xf\n"
    "v_mov_b32 v24, v22\n"
    "v_cvt_pk_bf16_f32 v66, v40, v41\n"
    "v_cvt_pk_bf16_f32 v67, v42, v43\n"
    "v_permlane16_swap_b32 v22, v24\n"
    "v_add_f32 v22, v22, v24\n"
    "v_mov_b32 v24, v22\n"
    "v_cvt_pk_bf16_f32 v68, v44, v45\n"
    "v_cvt_pk_bf16_f32 v69, v46, v47\n"
    "v_permlane32_swap_b32 v22, v24\n"
    "v_add_f32 v22, v22, v24\n"
    "v_mul_f32 v22, 0xbae38e39, v22\n"
    "v_pk_add_f32 v[28:29], v[28:29], v[22:23] op_sel_hi:[1,0]\n"
    "v_pk_add_f32 v[30:31], v[30:31], v[22:23] op_sel_hi:[1,0]\n"
    "v_pk_add_f32 v[32:33], v[32:33], v[22:23] op_sel_hi:[1,0]\n"
    "v_pk_add_f32 v[34:35], v[34:35], v[22:23] op_sel_hi:[1,0]\n"
    "v_pk_add_f32 v[36:37], v[36:37], v[22:23] op_sel_hi:[1,0]\n"
    "v_pk_add_f32 v[38:39], v[38:39], v[22:23] op_sel_hi:[1,0]\n"
    "v_pk_add_f32 v[40:41], v[40:41], v[22:23] op_sel_hi:[1,0]\n"
    "v_pk_add_f32 v[42:43], v[42:43], v[22:23] op_sel_hi:[1,0]\n"
    "v_pk_add_f32 v[44:45], v[44:45], v[22:23] op_sel_hi:[1,0]\n"
    "v_pk_add_f32 v[46:47], v[46:47], v[22:23] op_sel_hi:[1,0]\n"
    "v_pk_mul_f32 v[22:23], v[44:45], v[44:45]\n"
    "v_pk_mul_f32 v[24:25], v[28:29], v[28:29]\n"
    "v_pk_fma_f32 v[22:23], v[46:47], v[46:47], v[22:23]\n"
    "v_pk_fma_f32 v[24:25], v[30:31], v[30:31], v[24:25]\n"
    "v_cndmask_b32 v22, 0, v22, s[68:69]\n"
    "v_cndmask_b32 v23, 0, v23, s[68:69]\n"
    "v_pk_fma_f32 v[22:23], v[32:33], v[32:33], v[22:23]\n"
    "v_pk_fma_f32 v[24:25], v[34:35], v[34:35], v[24:25]\n"
    "v_pk_fma_f32 v[22:23], v[36:37], v[36:37], v[22:23]\n"
    "v_pk_fma_f32 v[24:25], v[38:39], v[38:39], v[24:25]\n"
    "v_pk_fma_f32 v[22:23], v[40:41], v[40:41], v[22:23]\n"
    "v_pk_fma_f32 v[24:25], v[42:43], v[42:43], v[24:25]\n"
    "v_pk_add_f32 v[22:23], v[22:23], v[24:25]\n"
    "v_add_f32 v22, v22, v23\n"
    "v_permlane16_swap_b32 v60, v62\n"
    "v_permlane16_swap_b32 v61, v63\n"
    "v_add_f32_dpp v22, v22, v22 quad_perm:[1,0,3,2] row_mask:0xf bank_mask:0xf\n"
    "v_permlane16_swap_b32 v64, v66\n"
    "v_permlane16_swap_b32 v65, v67\n"
    "v_add_f32_dpp v22, v22, v22 quad_perm:[2,3,0,1] row_mask:0xf bank_mask:0xf\n"
    "v_permlane16_swap_b32 v68, v70\n"
    "v_permlane16_swap_b32 v69, v71\n"
    "v_add_f32_dpp v22, v22, v22 row_half_mirror row_mask:0xf bank_mask:0xf\n"
    "v_mov_b32 v24, v22\n"
    "s_nop 1\n"
    "v_permlane16_swap_b32 v22, v24\n"
    "v_add_f32 v22, v22, v24\n"
    "v_mov_b32 v24, v22\n"
    "s_nop 1\n"
    "v_permlane32_swap_b32 v22, v24\n"
    "v_add_f32 v22, v22, v24\n"
    "v_mov_b32 v24, 0x3727c5ac\n"
    "v_fmac_f32 v24, 0x3ae38e39, v22\n"
    "v_rsq_f32 v22, v24\n"
    "s_nop 0\n"
    "v_pk_mul_f32 v[28:29], v[28:29], v[22:23] op_sel_hi:[1,0]\n"
    "v_pk_mul_f32 v[30:31], v[30:31], v[22:23] op_sel_hi:[1,0]\n"
    "v_cvt_pk_bf16_f32 v72, v28, v29\n"
    "v_cvt_pk_bf16_f32 v73, v30, v31\n"
    "v_pk_mul_f32 v[32:33], v[32:33], v[22:23] op_sel_hi:[1,0]\n"
    "v_pk_mul_f32 v[34:35], v[34:35], v[22:23] op_sel_hi:[1,0]\n"
    "v_cvt_pk_bf16_f32 v74, v32, v33\n"
    "v_cvt_pk_bf16_f32 v75, v34, v35\n"
    "v_pk_mul_f32 v[36:37], v[36:37], v[22:23] op_sel_hi:[1,0]\n"
    "v_pk_mul_f32 v[38:39], v[38:39], v[22:23] op_sel_hi:[1,0]\n"
    "v_cvt_pk_bf16_f32 v76, v36, v37\n"
    "v_cvt_pk_bf16_f32 v77, v38, v39\n"
    "v_permlane16_swap_b32 v72, v74\n"
    "v_permlane16_swap_b32 v73, v75\n"
    "buffer_store_dwordx4 v[72:75], v4, s[36:39], s61 offen offset:0 sc1\n"
    "v_pk_mul_f32 v[40:41], v[40:41], v[22:23] op_sel_hi:[1,0]\n"
    "v_pk_mul_f32 v[42:43], v[42:43], v[22:23] op_sel_hi:[1,0]\n"
    "v_cvt_pk_bf16_f32 v78, v40, v41\n"
    "v_cvt_pk_bf16_f32 v79, v42, v43\n"
    "v_pk_mul_f32 v[44:45], v[44:45], v[22:23] op_sel_hi:[1,0]\n"
    "v_pk_mul_f32 v[46:47], v[46:47], v[22:23] op_sel_hi:[1,0]\n"
    "v_cvt_pk_bf16_f32 v80, v44, v45\n"
    "v_cvt_pk_bf16_f32 v81, v46, v47\n"
    "v_permlane16_swap_b32 v76, v78\n"
    "v_permlane16_swap_b32 v77, v79\n"
    "buffer_store_dwordx4 v[76:79], v4, s[36:39], s61 offen offset:512 sc1\n"
    "v_permlane16_swap_b32 v80, v82\n"
    "v_permlane16_swap_b32 v81, v83\n"
    "buffer_store_dwordx4 v[80:83], v5, s[36:39], s61 offen offset:1024 sc1\n"
    "buffer_store_dwordx4 v[60:63], v2, s[44:47], s71 offen offset:0 sc1\n"
    "buffer_store_dwordx4 v[64:67], v2, s[44:47], s71 offen offset:512 sc1\n"
    "buffer_store_dwordx4 v[68:71], v3, s[44:47], s71 offen offset:1024 sc1\n"
    "L_pub%=:\n"
    "s_waitcnt vmcnt(0)\n"
    "s_barrier\n"
    "v_mov_b32 v21, s60\n"
    "s_mov_b64 exec, s[66:67]\n"
    "buffer_store_dword v21, off, s[36:39], s55 sc1\n"
    "s_mov_b64 exec, -1\n"
    "L_next%=:\n"
    "s_add_i32 s61, s61, 0x4000\n"
    "s_add_i32 s70, s70, s48\n"
    "s_add_i32 s71, s71, s49\n"
    "s_add_i32 s60, s60, 1\n"
    "s_cmp_lt_u32 s60, 64\n"
    "s_cbranch_scc1 L_job%=\n"
    "s_branch L_end%=\n"
    "L_last%=:\n"
    "v_cvt_pk_bf16_f32 v60, v28, v29\n"
    "v_cvt_pk_bf16_f32 v61, v30, v31\n"
    "v_cvt_pk_bf16_f32 v62, v32, v33\n"
    "v_cvt_pk_bf16_f32 v63, v34, v35\n"
    "v_cvt_pk_bf16_f32 v64, v36, v37\n"
    "v_cvt_pk_bf16_f32 v65, v38, v39\n"
    "v_cvt_pk_bf16_f32 v66, v40, v41\n"
    "v_cvt_pk_bf16_f32 v67, v42, v43\n"
    "v_cvt_pk_bf16_f32 v68, v44, v45\n"
    "v_cvt_pk_bf16_f32 v69, v46, v47\n"
    "v_permlane16_swap_b32 v60, v62\n"
    "v_permlane16_swap_b32 v61, v63\n"
    "buffer_store_dwordx4 v[60:63], v2, s[44:47], s71 offen offset:0\n"
    "v_permlane16_swap_b32 v64, v66\n"
    "v_permlane16_swap_b32 v65, v67\n"
    "buffer_store_dwordx4 v[64:67], v2, s[44:47], s71 offen offset:512\n"
    "v_permlane16_swap_b32 v68, v70\n"
    "v_permlane16_swap_b32 v69, v71\n"
    "buffer_store_dwordx4 v[68:71], v3, s[44:47], s71 offen offset:1024\n"
    "s_branch L_pub%=\n"
    "L_end%=:\n"
    "s_waitcnt vmcnt(0)\n"
    : "+{s[52:59]}"(s1)
      : "{s[36:51]}"(s0), "{v[0:7]}"(c0), "{v[8:11]}"(sel0), "{v[16:19]}"(sel1), "{v[84:87]}"(c1)
    : "v20", "v21", "v22", "v23", "v24", "v25", "v26", "v27", "v28", "v29", "v30", "v31", "v32", "v33", "v34", "v35", "v36", "v37", "v38", "v39",
      "v40", "v41", "v42", "v43", "v44", "v45", "v46", "v47", "v48", "v49", "v50", "v51", "v52", "v53", "v54", "v55", "v56", "v57", "v58", "v59",
      "v60", "v61", "v62", "v63", "v64", "v65", "v66", "v67", "v68", "v69", "v70", "v71", "v72", "v73", "v74", "v75", "v76", "v77", "v78", "v79",
      "v80", "v81", "v82", "v83", "a0", "a1", "a2", "a3", "a4", "a5", "a6", "a7", "a8", "a9", "a10", "a11", "a12", "a13", "a14", "a15",
      "a16", "a17", "a18", "a19", "a20", "a21", "a22", "a23", "a24", "a25", "a26", "a27", "a28", "a29", "a30", "a31", "a32", "a33", "a34", "a35",
      "s60", "s61", "s62", "s63", "s64", "s65", "s66", "s67", "s68", "s69", "s70", "s71", "vcc", "scc", "memory");
  __builtin_amdgcn_endpgm();
}

DI void attn_role(const Params& p, Ctx& c, LAS char* L, int idx) {
  const int lane = c.lane, l = idx >> 3, h = (idx & 7) * 4 + c.w, node = N_AT + idx;
  const int tok = lane & 15, g = lane >> 4;
  if (idx == 0 && c.w == 0) {
    const unsigned off = OFF_MSG + 1572864u;
    unsigned best0 = 0xFFFFFFFFu, best1 = 0xFFFFFFFFu, vo = lane < 8 ? (unsigned)lane * 16u : OOB;
    for (int rep = 0; rep < NPROBE; ++rep) {
      unsigned t0 = cyclk();
      st4((v4u){0u, 0u, 0u, 0u}, c.ws, vo, off + (unsigned)(rep & 1) * 256u);
      WAITV(0);
      unsigned t1 = cyclk();
      st4((v4u){0u, 0u, 0u, 0u}, c.ws, vo, off + 8192u + (unsigned)(rep & 1) * 256u);
      WAITV(0);
      best0 = min(best0, t1 - t0); best1 = min(best1, cyclk() - t1);
    }
    st1(((best1 < best0 ? 1u : 0u) ^ (off >> 21) ^ (__builtin_amdgcn_s_getreg(20 | (3 << 11)) >> 2)) & 1u, c.ws, lane == 0 ? 0u : OOB, OFF_CTL);
  }
  if (idx == 0) BAR();
  get_par(c);
  { const unsigned page = near(c, OFF_MSG + (unsigned)l * PG, DIE_CH(l));
    for (int i = 0; i < 22; ++i) {
      unsigned n = (unsigned)(i * 32 + (idx & 7) * 4 + c.w); v4u d = {~0u, ~0u, ~0u, ~0u};
      int f = (int)n - 576;
      if (f >= 0 && f < 9) {
        const unsigned short* m0 = (const unsigned short*)p.mem0 + (4 * ((lane >> 3) & 1) + (lane & 3)) * DM + 64 * f + 16 * g + 8 * ((lane >> 2) & 1);
        d = (v4u){m0[0] | ((unsigned)m0[2] << 16), m0[4] | ((unsigned)m0[6] << 16), m0[1] | ((unsigned)m0[3] << 16), m0[5] | ((unsigned)m0[7] << 16)};
      }
      st4(d, c.ws, (unsigned)lane * 16u, page + (n >> 3) * 16384u + (n & 7u) * 1024u);
    }
    WAITV(0); BAR(); publish(c, node, 1, 0); }
  const unsigned fb = (lane & 16) ? flagoff(c, N_WO + ((idx & 7) >> 1) * 6 + (lane & 15) % 6, l)
                    : ((lane & 8) ? flagoff(c, N_QKV + 8 * (idx & 7) + (lane & 7), l) : flagoff(c, N_CH + l * 32 + 4 * (idx & 7) + (lane & 3), 0));
  v4u c0, s0; v8u s1, mk8;
  c0[0] = (unsigned)lane * 16u; c0[1] = (unsigned)(16 * g + 8 + (lane & 7)) * 16u; c0[2] = fb; c0[3] = (lane & 16) ? 0u - RB : (unsigned)((lane >> 3) & 1);
  unsigned long long wp = (unsigned long long)c.wsp;
  s0[0] = (unsigned)wp; s0[1] = (unsigned)(wp >> 32) & 0xFFFFu; s0[2] = WS_USED; s0[3] = 0x00020000u;
  s1[0] = (unsigned)wp; s1[1] = (unsigned)(wp >> 32); s1[2] = head_base(c, l, h); s1[3] = kvm_base(c, l, h);
  s1[4] = near(c, OFF_O + (unsigned)l * LSTR + (unsigned)(h >> 3) * 1048576u, ODIE) + (unsigned)(((h & 7) >> 1) * 16384 + (h & 1) * 4096); s1[5] = flagoff(c, node, 0); s1[6] = (unsigned)c.deadline; s1[7] = (unsigned)c.w;
  #pragma unroll
  for (int j = 0; j < 4; ++j) { unsigned long long m = __ballot(4 * g + j <= tok); mk8[2 * j] = (unsigned)m; mk8[2 * j + 1] = (unsigned)(m >> 32); }
  uni<8>(s1);
  asm volatile(
    "s_nop 4\n"
    "s_mov_b32 s40, s36\n"
    "s_mov_b32 s41, s37\n"
    "s_mov_b32 s42, s38\n"
    "s_mov_b32 s43, s39\n"
    "s_lshr_b32 s68, 2, s51\n"
    "s_and_b32 s68, s68, 1\n"
    "s_mov_b32 s69, 0\n"
    "s_mov_b32 s60, -1\n"
    "s_mov_b32 s61, 0\n"
    "v_mov_b32 v6, 0xff800000\n"
    "v_mov_b64 v[46:47], 0\n"
    "s_mov_b32 s62, -1\n"
    "s_mov_b32 s65, s46\n"
    "s_cmp_lg_u32 s51, 0\n"
    "s_cbranch_scc1 L_goti%=\n"
    "v_add_u32 v5, s62, v3\n"
    "v_min_i32 v5, 63, v5\n"
    "L_polli%=:\n"
    "global_load_dword v4, v2, s[44:45] sc1\n"
    "s_waitcnt vmcnt(0)\n"
    "v_cmp_gt_i32 vcc, v5, v4\n"
    "s_cbranch_vccz L_goti%=\n"
    "s_memrealtime s[66:67]\n"
    "s_waitcnt lgkmcnt(0)\n"
    "s_sub_i32 s63, s50, s66\n"
    "s_cmp_lt_i32 s63, 0\n"
    "s_cbranch_scc1 L_goti%=\n"
    "s_sleep 2\n"
    "s_branch L_polli%=\n"
    "L_goti%=:\n"
    "s_barrier\n"
    "s_add_i32 s64, s65, 0\n"
    "buffer_load_dwordx4 a[0:3], v0, s[40:43], s64 offen offset:0 sc1\n"
    "buffer_load_dwordx4 a[4:7], v0, s[40:43], s64 offen offset:1024 sc1\n"
    "buffer_load_dwordx4 a[8:11], v0, s[40:43], s64 offen offset:2048 sc1\n"
    "buffer_load_dwordx4 a[12:15], v0, s[40:43], s64 offen offset:3072 sc1\n"
    "s_add_i32 s64, s65, 4096\n"
    "buffer_load_dwordx4 a[16:19], v0, s[40:43], s64 offen offset:0 sc1\n"
    "buffer_load_dwordx4 a[20:23], v0, s[40:43], s64 offen offset:1024 sc1\n"
    "buffer_load_dwordx4 a[24:27], v0, s[40:43], s64 offen offset:2048 sc1\n"
    "buffer_load_dwordx4 a[28:31], v0, s[40:43], s64 offen offset:3072 sc1\n"
    "s_add_i32 s64, s65, 16384\n"
    "buffer_load_dwordx4 a[32:35], v0, s[40:43], s64 offen offset:0 sc1\n"
    "buffer_load_dwordx4 a[36:39], v0, s[40:43], s64 offen offset:1024 sc1\n"
    "buffer_load_dwordx4 a[40:43], v0, s[40:43], s64 offen offset:2048 sc1\n"
    "buffer_load_dwordx4 a[44:47], v0, s[40:43], s64 offen offset:3072 sc1\n"
    "s_add_i32 s64, s65, 20480\n"
    "buffer_load_dwordx4 a[48:51], v0, s[40:43], s64 offen offset:0 sc1\n"
    "buffer_load_dwordx4 a[52:55], v0, s[40:43], s64 offen offset:1024 sc1\n"
    "buffer_load_dwordx4 a[56:59], v0, s[40:43], s64 offen offset:2048 sc1\n"
    "buffer_load_dwordx4 a[60:63], v0, s[40:43], s64 offen offset:3072 sc1\n"
    "s_add_i32 s64, s65, 32768\n"
    "buffer_load_dwordx4 a[64:67], v0, s[40:43], s64 offen offset:0 sc1\n"
    "buffer_load_dwordx4 a[68:71], v0, s[40:43], s64 offen offset:1024 sc1\n"
    "buffer_load_dwordx4 a[72:75], v0, s[40:43], s64 offen offset:2048 sc1\n"
    "buffer_load_dwordx4 a[76:79], v0, s[40:43], s64 offen offset:3072 sc1\n"
    "s_add_i32 s64, s65, 36864\n"
    "buffer_load_dwordx4 a[80:83], v0, s[40:43], s64 offen offset:0 sc1\n"
    "buffer_load_dwordx4 a[84:87], v0, s[40:43], s64 offen offset:1024 sc1\n"
    "buffer_load_dwordx4 a[88:91], v0, s[40:43], s64 offen offset:2048 sc1\n"
    "buffer_load_dwordx4 a[92:95], v0, s[40:43], s64 offen offset:3072 sc1\n"
    "s_mov_b32 s62, 0\n"
    "L_job0%=:\n"
    "s_cmp_lt_u32 s62, 63\n"
    "s_cselect_b32 s42, s38, 0\n"
    "s_add_i32 s65, s62, 1\n"
    "s_and_b32 s65, s65, 15\n"
    "s_mul_i32 s65, s65, 0xc000\n"
    "s_add_i32 s65, s65, s46\n"
    "s_and_b32 s70, s62, 7\n"
    "s_lshl_b32 s70, s70, 14\n"
    "s_add_i32 s70, s70, s47\n"
    "s_and_b32 s71, s62, 7\n"
    "s_lshl_b32 s71, s71, 16\n"
    "s_add_i32 s71, s71, s48\n"
    "s_add_i32 s64, s70, 0x1000\n"
    "s_cmp_lg_u32 s51, 0\n"
    "s_cbranch_scc1 L_got0%=\n"
    "v_add_u32 v5, s62, v3\n"
    "v_min_i32 v5, 63, v5\n"
    "L_poll0%=:\n"
    "global_load_dword v4, v2, s[44:45] sc1\n"
    "s_waitcnt vmcnt(0)\n"
    "v_cmp_gt_i32 vcc, v5, v4\n"
    "s_cbranch_vccz L_got0%=\n"
    "s_memrealtime s[66:67]\n"
    "s_waitcnt lgkmcnt(0)\n"
    "s_sub_i32 s63, s50, s66\n"
    "s_cmp_lt_i32 s63, 0\n"
    "s_cbranch_scc1 L_got0%=\n"
    "s_sleep 2\n"
    "s_branch L_poll0%=\n"
    "L_got0%=:\n"
    "s_barrier\n"
    "buffer_load_dwordx4 a[192:195], v1, s[36:39], s70 offen offset:0 sc1\n"
    "buffer_load_dwordx4 a[196:199], v1, s[36:39], s70 offen offset:1024 sc1\n"
    "buffer_load_dwordx4 a[200:203], v1, s[36:39], s70 offen offset:2048 sc1\n"
    "buffer_load_dwordx4 a[204:207], v1, s[36:39], s70 offen offset:3072 sc1\n"
    "buffer_load_dwordx4 a[208:211], v0, s[36:39], s64 offen offset:0 sc1\n"
    "buffer_load_dwordx4 a[212:215], v0, s[36:39], s64 offen offset:1024 sc1\n"
    "buffer_load_dwordx4 a[216:219], v0, s[36:39], s64 offen offset:2048 sc1\n"
    "buffer_load_dwordx4 a[220:223], v0, s[36:39], s64 offen offset:3072 sc1\n"
    "s_waitcnt vmcnt(8)\n"
    "v_mfma_f32_16x16x32_bf16 v[16:19], a[32:35], a[0:3], 0\n"
    "v_mfma_f32_16x16x32_bf16 v[20:23], a[32:35], a[4:7], 0\n"
    "v_mfma_f32_16x16x32_bf16 v[24:27], a[36:39], a[4:7], 0\n"
    "v_mfma_f32_16x16x32_bf16 v[16:19], a[40:43], a[8:11], v[16:19]\n"
    "v_mfma_f32_16x16x32_bf16 v[20:23], a[40:43], a[12:15], v[20:23]\n"
    "v_mfma_f32_16x16x32_bf16 v[24:27], a[44:47], a[12:15], v[24:27]\n"
    "s_add_i32 s64, s65, 0\n"
    "buffer_load_dwordx4 a[96:99], v0, s[40:43], s64 offen offset:0 sc1\n"
    "v_mfma_f32_16x16x32_bf16 v[16:19], a[48:51], a[16:19], v[16:19]\n"
    "v_mfma_f32_16x16x32_bf16 v[20:23], a[48:51], a[20:23], v[20:23]\n"
    "v_mfma_f32_16x16x32_bf16 v[24:27], a[52:55], a[20:23], v[24:27]\n"
    "buffer_load_dwordx4 a[100:103], v0, s[40:43], s64 offen offset:1024 sc1\n"
    "v_mfma_f32_16x16x32_bf16 v[16:19], a[56:59], a[24:27], v[16:19]\n"
    "v_mfma_f32_16x16x32_bf16 v[20:23], a[56:59], a[28:31], v[20:23]\n"
    "v_mfma_f32_16x16x32_bf16 v[24:27], a[60:63], a[28:31], v[24:27]\n"
    "buffer_load_dwordx4 a[104:107], v0, s[40:43], s64 offen offset:2048 sc1\n"
    "s_nop 4\n"
    "v_cndmask_b32 v16, v6, v16, s[52:53]\n"
    "v_cndmask_b32 v17, v6, v17, s[54:55]\n"
    "v_cndmask_b32 v18, v6, v18, s[56:57]\n"
    "v_cndmask_b32 v19, v6, v19, s[58:59]\n"
    "v_cndmask_b32 v24, v6, v24, s[52:53]\n"
    "v_cndmask_b32 v25, v6, v25, s[54:55]\n"
    "v_cndmask_b32 v26, v6, v26, s[56:57]\n"
    "v_cndmask_b32 v27, v6, v27, s[58:59]\n"
    "buffer_load_dwordx4 a[108:111], v0, s[40:43], s64 offen offset:3072 sc1\n"
    "v_max3_f32 v36, v16, v17, v18\n"
    "v_max3_f32 v37, v20, v21, v22\n"
    "v_max_f32 v36, v36, v19\n"
    "v_max3_f32 v37, v37, v23, v24\n"
    "v_max3_f32 v37, v37, v25, v26\n"
    "v_max_f32 v37, v37, v27\n"
    "s_add_i32 s64, s65, 4096\n"
    "buffer_load_dwordx4 a[112:115], v0, s[40:43], s64 offen offset:0 sc1\n"
    "buffer_load_dwordx4 a[116:119], v0, s[40:43], s64 offen offset:1024 sc1\n"
    "s_waitcnt vmcnt(13)\n"
    "v_mfma_f32_16x16x32_bf16 v[28:31], a[192:195], a[0:3], 0\n"
    "v_mfma_f32_16x16x32_bf16 v[32:35], a[192:195], a[4:7], 0\n"
    "s_waitcnt vmcnt(12)\n"
    "v_mfma_f32_16x16x32_bf16 v[28:31], a[196:199], a[8:11], v[28:31]\n"
    "v_mfma_f32_16x16x32_bf16 v[32:35], a[196:199], a[12:15], v[32:35]\n"
    "s_waitcnt vmcnt(11)\n"
    "v_mfma_f32_16x16x32_bf16 v[28:31], a[200:203], a[16:19], v[28:31]\n"
    "v_mfma_f32_16x16x32_bf16 v[32:35], a[200:203], a[20:23], v[32:35]\n"
    "s_waitcnt vmcnt(10)\n"
    "v_mfma_f32_16x16x32_bf16 v[28:31], a[204:207], a[24:27], v[28:31]\n"
    "v_mfma_f32_16x16x32_bf16 v[32:35], a[204:207], a[28:31], v[32:35]\n"
    "buffer_load_dwordx4 a[120:123], v0, s[40:43], s64 offen offset:2048 sc1\n"
    "s_nop 2\n"
    "v_max3_f32 v36, v36, v28, v29\n"
    "v_max3_f32 v36, v36, v30, v31\n"
    "v_mov_b32 v38, v36\n"
    "s_nop 0\n"
    "v_max3_f32 v37, v37, v32, v33\n"
    "v_max3_f32 v37, v37, v34, v35\n"
    "v_mov_b32 v39, v37\n"
    "v_permlane16_swap_b32 v36, v38\n"
    "v_max_f32 v36, v36, v38\n"
    "v_mov_b32 v38, v36\n"
    "v_permlane16_swap_b32 v37, v39\n"
    "v_max_f32 v37, v37, v39\n"
    "v_mov_b32 v39, v37\n"
    "v_permlane32_swap_b32 v36, v38\n"
    "v_max_f32 v36, v36, v38\n"
    "v_mul_f32 v36, 0xbe0293ee, v36\n"
    "v_permlane32_swap_b32 v37, v39\n"
    "v_max_f32 v37, v37, v39\n"
    "v_mul_f32 v37, 0xbe0293ee, v37\n"
    "buffer_load_dwordx4 a[124:127], v0, s[40:43], s64 offen offset:3072 sc1\n"
    "v_fmamk_f32 v16, v16, 0x3e0293ee, v36\n"
    "v_fmamk_f32 v17, v17, 0x3e0293ee, v36\n"
    "v_exp_f32 v16, v16\n"
    "v_fmamk_f32 v18, v18, 0x3e0293ee, v36\n"
    "v_exp_f32 v17, v17\n"
    "v_fmamk_f32 v19, v19, 0x3e0293ee, v36\n"
    "v_exp_f32 v18, v18\n"
    "s_add_i32 s64, s65, 16384\n"
    "buffer_load_dwordx4 a[128:131], v0, s[40:43], s64 offen offset:0 sc1\n"
    "v_fmamk_f32 v28, v28, 0x3e0293ee, v36\n"
    "v_exp_f32 v19, v19\n"
    "v_fmamk_f32 v29, v29, 0x3e0293ee, v36\n"
    "v_exp_f32 v28, v28\n"
    "v_fmamk_f32 v30, v30, 0x3e0293ee, v36\n"
    "v_exp_f32 v29, v29\n"
    "v_fmamk_f32 v31, v31, 0x3e0293ee, v36\n"
    "v_exp_f32 v30, v30\n"
    "buffer_load_dwordx4 a[132:135], v0, s[40:43], s64 offen offset:1024 sc1\n"
    "v_fmamk_f32 v20, v20, 0x3e0293ee, v37\n"
    "v_exp_f32 v31, v31\n"
    "v_fmamk_f32 v21, v21, 0x3e0293ee, v37\n"
    "v_exp_f32 v20, v20\n"
    "v_fmamk_f32 v22, v22, 0x3e0293ee, v37\n"
    "v_exp_f32 v21, v21\n"
    "v_fmamk_f32 v23, v23, 0x3e0293ee, v37\n"
    "v_exp_f32 v22, v22\n"
    "buffer_load_dwordx4 a[136:139], v0, s[40:43], s64 offen offset:2048 sc1\n"
    "v_fmamk_f32 v24, v24, 0x3e0293ee, v37\n"
    "v_exp_f32 v23, v23\n"
    "v_fmamk_f32 v25, v25, 0x3e0293ee, v37\n"
    "v_exp_f32 v24, v24\n"
    "v_fmamk_f32 v26, v26, 0x3e0293ee, v37\n"
    "v_exp_f32 v25, v25\n"
    "v_fmamk_f32 v27, v27, 0x3e0293ee, v37\n"
    "v_exp_f32 v26, v26\n"
    "buffer_load_dwordx4 a[140:143], v0, s[40:43], s64 offen offset:3072 sc1\n"
    "v_fmamk_f32 v32, v32, 0x3e0293ee, v37\n"
    "v_exp_f32 v27, v27\n"
    "v_fmamk_f32 v33, v33, 0x3e0293ee, v37\n"
    "v_exp_f32 v32, v32\n"
    "v_fmamk_f32 v34, v34, 0x3e0293ee, v37\n"
    "v_exp_f32 v33, v33\n"
    "v_fmamk_f32 v35, v35, 0x3e0293ee, v37\n"
    "v_exp_f32 v34, v34\n"
    "s_add_i32 s64, s65, 20480\n"
    "buffer_load_dwordx4 a[144:147], v0, s[40:43], s64 offen offset:0 sc1\n"
    "v_exp_f32 v35, v35\n"
    "v_pk_add_f32 v[40:41], v[16:17], v[18:19]\n"
    "v_pk_add_f32 v[42:43], v[28:29], v[30:31]\n"
    "v_pk_add_f32 v[38:39], v[20:21], v[22:23]\n"
    "v_pk_add_f32 v[36:37], v[24:25], v[26:27]\n"
    "v_add_f32 v40, v40, v41\n"
    "v_add_f32 v42, v42, v43\n"
    "v_pk_add_f32 v[38:39], v[38:39], v[36:37]\n"
    "v_pk_add_f32 v[36:37], v[32:33], v[34:35]\n"
    "v_cndmask_b32 v42, 0, v42, s[60:61]\n"
    "v_add_f32 v41, v38, v39\n"
    "v_add_f32 v43, v36, v37\n"
    "v_add_f32 v40, v40, v42\n"
    "v_cndmask_b32 v43, 0, v43, s[60:61]\n"
    "v_mov_b32 v38, v40\n"
    "v_add_f32 v41, v41, v43\n"
    "v_mov_b32 v39, v41\n"
    "v_permlane16_swap_b32 v40, v38\n"
    "v_add_f32 v40, v40, v38\n"
    "v_mov_b32 v38, v40\n"
    "v_permlane16_swap_b32 v41, v39\n"
    "v_add_f32 v41, v41, v39\n"
    "v_mov_b32 v39, v41\n"
    "v_permlane32_swap_b32 v40, v38\n"
    "v_add_f32 v40, v40, v38\n"
    "v_rcp_f32 v40, v40\n"
    "v_permlane32_swap_b32 v41, v39\n"
    "v_add_f32 v42, v41, v39\n"
    "v_rcp_f32 v42, v42\n"
    "buffer_load_dwordx4 a[148:151], v0, s[40:43], s64 offen offset:1024 sc1\n"
    "v_pk_mul_f32 v[16:17], v[16:17], v[40:41] op_sel_hi:[1,0]\n"
    "v_pk_mul_f32 v[18:19], v[18:19], v[40:41] op_sel_hi:[1,0]\n"
    "v_pk_mul_f32 v[28:29], v[28:29], v[40:41] op_sel_hi:[1,0]\n"
    "v_pk_mul_f32 v[30:31], v[30:31], v[40:41] op_sel_hi:[1,0]\n"
    "v_cvt_pk_bf16_f32 v44, v16, v17\n"
    "v_cvt_pk_bf16_f32 v45, v18, v19\n"
    "v_cvt_pk_bf16_f32 v52, v28, v29\n"
    "v_cvt_pk_bf16_f32 v53, v30, v31\n"
    "s_waitcnt vmcnt(14)\n"
    "v_pk_mul_f32 v[20:21], v[20:21], v[42:43] op_sel_hi:[1,0]\n"
    "v_pk_mul_f32 v[22:23], v[22:23], v[42:43] op_sel_hi:[1,0]\n"
    "v_pk_mul_f32 v[24:25], v[24:25], v[42:43] op_sel_hi:[1,0]\n"
    "v_pk_mul_f32 v[26:27], v[26:27], v[42:43] op_sel_hi:[1,0]\n"
    "v_pk_mul_f32 v[32:33], v[32:33], v[42:43] op_sel_hi:[1,0]\n"
    "v_pk_mul_f32 v[34:35], v[34:35], v[42:43] op_sel_hi:[1,0]\n"
    "s_add_i32 s63, s71, 0x80000\n"
    "v_mfma_f32_16x16x32_bf16 v[56:59], a[64:67], v[44:47], 0\n"
    "v_cvt_pk_bf16_f32 v48, v20, v21\n"
    "v_cvt_pk_bf16_f32 v49, v22, v23\n"
    "v_mfma_f32_16x16x32_bf16 v[60:63], a[68:71], v[44:47], 0\n"
    "v_cvt_pk_bf16_f32 v50, v24, v25\n"
    "v_cvt_pk_bf16_f32 v51, v26, v27\n"
    "v_mfma_f32_16x16x16_bf16 v[56:59], a[208:209], v[52:53], v[56:59]\n"
    "v_cvt_pk_bf16_f32 v54, v32, v33\n"
    "v_cvt_pk_bf16_f32 v55, v34, v35\n"
    "v_mfma_f32_16x16x16_bf16 v[60:63], a[210:211], v[52:53], v[60:63]\n"
    "v_mfma_f32_16x16x32_bf16 v[72:75], a[72:75], v[44:47], 0\n"
    "v_mfma_f32_16x16x32_bf16 v[76:79], a[76:79], v[44:47], 0\n"
    "v_cvt_pk_bf16_f32 v88, v56, v57\n"
    "v_cvt_pk_bf16_f32 v89, v58, v59\n"
    "v_mfma_f32_16x16x16_bf16 v[72:75], a[212:213], v[52:53], v[72:75]\n"
    "v_cvt_pk_bf16_f32 v90, v60, v61\n"
    "v_cvt_pk_bf16_f32 v91, v62, v63\n"
    "v_mfma_f32_16x16x16_bf16 v[76:79], a[214:215], v[52:53], v[76:79]\n"
    "buffer_store_dwordx4 v[88:91], v0, s[36:39], s71 offen offset:0 sc1\n"
    "v_mfma_f32_16x16x32_bf16 v[64:67], a[64:67], v[48:51], 0\n"
    "v_mfma_f32_16x16x32_bf16 v[68:71], a[68:71], v[48:51], 0\n"
    "v_cvt_pk_bf16_f32 v96, v72, v73\n"
    "v_cvt_pk_bf16_f32 v97, v74, v75\n"
    "v_mfma_f32_16x16x16_bf16 v[64:67], a[208:209], v[54:55], v[64:67]\n"
    "v_cvt_pk_bf16_f32 v98, v76, v77\n"
    "v_cvt_pk_bf16_f32 v99, v78, v79\n"
    "v_mfma_f32_16x16x16_bf16 v[68:71], a[210:211], v[54:55], v[68:71]\n"
    "buffer_store_dwordx4 v[96:99], v0, s[36:39], s71 offen offset:1024 sc1\n"
    "v_mfma_f32_16x16x32_bf16 v[80:83], a[72:75], v[48:51], 0\n"
    "v_mfma_f32_16x16x32_bf16 v[84:87], a[76:79], v[48:51], 0\n"
    "v_cvt_pk_bf16_f32 v92, v64, v65\n"
    "v_cvt_pk_bf16_f32 v93, v66, v67\n"
    "v_mfma_f32_16x16x16_bf16 v[80:83], a[212:213], v[54:55], v[80:83]\n"
    "v_cvt_pk_bf16_f32 v94, v68, v69\n"
    "v_cvt_pk_bf16_f32 v95, v70, v71\n"
    "v_mfma_f32_16x16x16_bf16 v[84:87], a[214:215], v[54:55], v[84:87]\n"
    "buffer_store_dwordx4 v[92:95], v0, s[36:39], s63 offen offset:0 sc1\n"
    "v_mfma_f32_16x16x32_bf16 v[56:59], a[80:83], v[44:47], 0\n"
    "v_mfma_f32_16x16x32_bf16 v[60:63], a[84:87], v[44:47], 0\n"
    "v_cvt_pk_bf16_f32 v100, v80, v81\n"
    "v_cvt_pk_bf16_f32 v101, v82, v83\n"
    "v_mfma_f32_16x16x16_bf16 v[56:59], a[216:217], v[52:53], v[56:59]\n"
    "v_cvt_pk_bf16_f32 v102, v84, v85\n"
    "v_cvt_pk_bf16_f32 v103, v86, v87\n"
    "v_mfma_f32_16x16x16_bf16 v[60:63], a[218:219], v[52:53], v[60:63]\n"
    "buffer_store_dwordx4 v[100:103], v0, s[36:39], s63 offen offset:1024 sc1\n"
    "v_mfma_f32_16x16x32_bf16 v[72:75], a[88:91], v[44:47], 0\n"
    "v_mfma_f32_16x16x32_bf16 v[76:79], a[92:95], v[44:47], 0\n"
    "v_cvt_pk_bf16_f32 v88, v56, v57\n"
    "v_cvt_pk_bf16_f32 v89, v58, v59\n"
    "v_mfma_f32_16x16x16_bf16 v[72:75], a[220:221], v[52:53], v[72:75]\n"
    "v_cvt_pk_bf16_f32 v90, v60, v61\n"
    "v_cvt_pk_bf16_f32 v91, v62, v63\n"
    "v_mfma_f32_16x16x16_bf16 v[76:79], a[222:223], v[52:53], v[76:79]\n"
    "buffer_store_dwordx4 v[88:91], v0, s[36:39], s71 offen offset:2048 sc1\n"
    "v_mfma_f32_16x16x32_bf16 v[64:67], a[80:83], v[48:51], 0\n"
    "v_mfma_f32_16x16x32_bf16 v[68:71], a[84:87], v[48:51], 0\n"
    "v_cvt_pk_bf16_f32 v96, v72, v73\n"
    "v_cvt_pk_bf16_f32 v97, v74, v75\n"
    "v_mfma_f32_16x16x16_bf16 v[64:67], a[216:217], v[54:55], v[64:67]\n"
    "v_cvt_pk_bf16_f32 v98, v76, v77\n"
    "v_cvt_pk_bf16_f32 v99, v78, v79\n"
    "v_mfma_f32_16x16x16_bf16 v[68:71], a[218:219], v[54:55], v[68:71]\n"
    "buffer_store_dwordx4 v[96:99], v0, s[36:39], s71 offen offset:3072 sc1\n"
    "v_mfma_f32_16x16x32_bf16 v[80:83], a[88:91], v[48:51], 0\n"
    "v_mfma_f32_16x16x32_bf16 v[84:87], a[92:95], v[48:51], 0\n"
    "v_cvt_pk_bf16_f32 v92, v64, v65\n"
    "v_cvt_pk_bf16_f32 v93, v66, v67\n"
    "v_mfma_f32_16x16x16_bf16 v[80:83], a[220:221], v[54:55], v[80:83]\n"
    "v_cvt_pk_bf16_f32 v94, v68, v69\n"
    "v_cvt_pk_bf16_f32 v95, v70, v71\n"
    "v_mfma_f32_16x16x16_bf16 v[84:87], a[222:223], v[54:55], v[84:87]\n"
    "buffer_store_dwordx4 v[92:95], v0, s[36:39], s63 offen offset:2048 sc1\n"
    "s_nop 2\n"
    "v_cvt_pk_bf16_f32 v100, v80, v81\n"
    "v_cvt_pk_bf16_f32 v101, v82, v83\n"
    "s_nop 1\n"
    "v_cvt_pk_bf16_f32 v102, v84, v85\n"
    "v_cvt_pk_bf16_f32 v103, v86, v87\n"
    "buffer_store_dwordx4 v[100:103], v0, s[36:39], s63 offen offset:3072 sc1\n"
    "s_barrier\n"
    "buffer_load_dwordx4 a[152:155], v0, s[40:43], s64 offen offset:2048 sc1\n"
    "buffer_load_dwordx4 a[156:159], v0, s[40:43], s64 offen offset:3072 sc1\n"
    "s_add_i32 s64, s65, 32768\n"
    "buffer_load_dwordx4 a[160:163], v0, s[40:43], s64 offen offset:0 sc1\n"
    "buffer_load_dwordx4 a[164:167], v0, s[40:43], s64 offen offset:1024 sc1\n"
    "buffer_load_dwordx4 a[168:171], v0, s[40:43], s64 offen offset:2048 sc1\n"
    "buffer_load_dwordx4 a[172:175], v0, s[40:43], s64 offen offset:3072 sc1\n"
    "s_add_i32 s64, s65, 36864\n"
    "buffer_load_dwordx4 a[176:179], v0, s[40:43], s64 offen offset:0 sc1\n"
    "buffer_load_dwordx4 a[180:183], v0, s[40:43], s64 offen offset:1024 sc1\n"
    "buffer_load_dwordx4 a[184:187], v0, s[40:43], s64 offen offset:2048 sc1\n"
    "buffer_load_dwordx4 a[188:191], v0, s[40:43], s64 offen offset:3072 sc1\n"
    "s_waitcnt vmcnt(10)\n"
    "s_barrier\n"
    "v_mov_b32 v5, s62\n"
    "s_mov_b64 exec, s[68:69]\n"
    "buffer_store_dword v5, off, s[36:39], s49 sc1\n"
    "s_mov_b64 exec, -1\n"
    "s_add_i32 s62, s62, 1\n"
    "L_job1%=:\n"
    "s_cmp_lt_u32 s62, 63\n"
    "s_cselect_b32 s42, s38, 0\n"
    "s_add_i32 s65, s62, 1\n"
    "s_and_b32 s65, s65, 15\n"
    "s_mul_i32 s65, s65, 0xc000\n"
    "s_add_i32 s65, s65, s46\n"
    "s_and_b32 s70, s62, 7\n"
    "s_lshl_b32 s70, s70, 14\n"
    "s_add_i32 s70, s70, s47\n"
    "s_and_b32 s71, s62, 7\n"
    "s_lshl_b32 s71, s71, 16\n"
    "s_add_i32 s71, s71, s48\n"
    "s_add_i32 s64, s70, 0x1000\n"
    "s_cmp_lg_u32 s51, 0\n"
    "s_cbranch_scc1 L_got1%=\n"
    "v_add_u32 v5, s62, v3\n"
    "v_min_i32 v5, 63, v5\n"
    "L_poll1%=:\n"
    "global_load_dword v4, v2, s[44:45] sc1\n"
    "s_waitcnt vmcnt(0)\n"
    "v_cmp_gt_i32 vcc, v5, v4\n"
    "s_cbranch_vccz L_got1%=\n"
    "s_memrealtime s[66:67]\n"
    "s_waitcnt lgkmcnt(0)\n"
    "s_sub_i32 s63, s50, s66\n"
    "s_cmp_lt_i32 s63, 0\n"
    "s_cbranch_scc1 L_got1%=\n"
    "s_sleep 2\n"
    "s_branch L_poll1%=\n"
    "L_got1%=:\n"
    "s_barrier\n"
    "buffer_load_dwordx4 a[192:195], v1, s[36:39], s70 offen offset:0 sc1\n"
    "buffer_load_dwordx4 a[196:199], v1, s[36:39], s70 offen offset:1024 sc1\n"
    "buffer_load_dwordx4 a[200:203], v1, s[36:39], s70 offen offset:2048 sc1\n"
    "buffer_load_dwordx4 a[204:207], v1, s[36:39], s70 offen offset:3072 sc1\n"
    "buffer_load_dwordx4 a[208:211], v0, s[36:39], s64 offen offset:0 sc1\n"
    "buffer_load_dwordx4 a[212:215], v0, s[36:39], s64 offen offset:1024 sc1\n"
    "buffer_load_dwordx4 a[216:219], v0, s[36:39], s64 offen offset:2048 sc1\n"
    "buffer_load_dwordx4 a[220:223], v0, s[36:39], s64 offen offset:3072 sc1\n"
    "s_waitcnt vmcnt(8)\n"
    "v_mfma_f32_16x16x32_bf16 v[16:19], a[128:131], a[96:99], 0\n"
    "v_mfma_f32_16x16x32_bf16 v[20:23], a[128:131], a[100:103], 0\n"
    "v_mfma_f32_16x16x32_bf16 v[24:27], a[132:135], a[100:103], 0\n"
    "v_mfma_f32_16x16x32_bf16 v[16:19], a[136:139], a[104:107], v[16:19]\n"
    "v_mfma_f32_16x16x32_bf16 v[20:23], a[136:139], a[108:111], v[20:23]\n"
    "v_mfma_f32_16x16x32_bf16 v[24:27], a[140:143], a[108:111], v[24:27]\n"
    "s_add_i32 s64, s65, 0\n"
    "buffer_load_dwordx4 a[0:3], v0, s[40:43], s64 offen offset:0 sc1\n"
    "v_mfma_f32_16x16x32_bf16 v[16:19], a[144:147], a[112:115], v[16:19]\n"
    "v_mfma_f32_16x16x32_bf16 v[20:23], a[144:147], a[116:119], v[20:23]\n"
    "v_mfma_f32_16x16x32_bf16 v[24:27], a[148:151], a[116:119], v[24:27]\n"
    "buffer_load_dwordx4 a[4:7], v0, s[40:43], s64 offen offset:1024 sc1\n"
    "v_mfma_f32_16x16x32_bf16 v[16:19], a[152:155], a[120:123], v[16:19]\n"
    "v_mfma_f32_16x16x32_bf16 v[20:23], a[152:155], a[124:127], v[20:23]\n"
    "v_mfma_f32_16x16x32_bf16 v[24:27], a[156:159], a[124:127], v[24:27]\n"
    "buffer_load_dwordx4 a[8:11], v0, s[40:43], s64 offen offset:2048 sc1\n"
    "s_nop 4\n"
    "v_cndmask_b32 v16, v6, v16, s[52:53]\n"
    "v_cndmask_b32 v17, v6, v17, s[54:55]\n"
    "v_cndmask_b32 v18, v6, v18, s[56:57]\n"
    "v_cndmask_b32 v19, v6, v19, s[58:59]\n"
    "v_cndmask_b32 v24, v6, v24, s[52:53]\n"
    "v_cndmask_b32 v25, v6, v25, s[54:55]\n"
    "v_cndmask_b32 v26, v6, v26, s[56:57]\n"
    "v_cndmask_b32 v27, v6, v27, s[58:59]\n"
    "buffer_load_dwordx4 a[12:15], v0, s[40:43], s64 offen offset:3072 sc1\n"
    "v_max3_f32 v36, v16, v17, v18\n"
    "v_max3_f32 v37, v20, v21, v22\n"
    "v_max_f32 v36, v36, v19\n"
    "v_max3_f32 v37, v37, v23, v24\n"
    "v_max3_f32 v37, v37, v25, v26\n"
    "v_max_f32 v37, v37, v27\n"
    "s_add_i32 s64, s65, 4096\n"
    "buffer_load_dwordx4 a[16:19], v0, s[40:43], s64 offen offset:0 sc1\n"
    "buffer_load_dwordx4 a[20:23], v0, s[40:43], s64 offen offset:1024 sc1\n"
    "s_waitcnt vmcnt(13)\n"
    "v_mfma_f32_16x16x32_bf16 v[28:31], a[192:195], a[96:99], 0\n"
    "v_mfma_f32_16x16x32_bf16 v[32:35], a[192:195], a[100:103], 0\n"
    "s_waitcnt vmcnt(12)\n"
    "v_mfma_f32_16x16x32_bf16 v[28:31], a[196:199], a[104:107], v[28:31]\n"
    "v_mfma_f32_16x16x32_bf16 v[32:35], a[196:199], a[108:111], v[32:35]\n"
    "s_waitcnt vmcnt(11)\n"
    "v_mfma_f32_16x16x32_bf16 v[28:31], a[200:203], a[112:115], v[28:31]\n"
    "v_mfma_f32_16x16x32_bf16 v[32:35], a[200:203], a[116:119], v[32:35]\n"
    "s_waitcnt vmcnt(10)\n"
    "v_mfma_f32_16x16x32_bf16 v[28:31], a[204:207], a[120:123], v[28:31]\n"
    "v_mfma_f32_16x16x32_bf16 v[32:35], a[204:207], a[124:127], v[32:35]\n"
    "buffer_load_dwordx4 a[24:27], v0, s[40:43], s64 offen offset:2048 sc1\n"
    "s_nop 2\n"
    "v_max3_f32 v36, v36, v28, v29\n"
    "v_max3_f32 v36, v36, v30, v31\n"
    "v_mov_b32 v38, v36\n"
    "s_nop 0\n"
    "v_max3_f32 v37, v37, v32, v33\n"
    "v_max3_f32 v37, v37, v34, v35\n"
    "v_mov_b32 v39, v37\n"
    "v_permlane16_swap_b32 v36, v38\n"
    "v_max_f32 v36, v36, v38\n"
    "v_mov_b32 v38, v36\n"
    "v_permlane16_swap_b32 v37, v39\n"
    "v_max_f32 v37, v37, v39\n"
    "v_mov_b32 v39, v37\n"
    "v_permlane32_swap_b32 v36, v38\n"
    "v_max_f32 v36, v36, v38\n"
    "v_mul_f32 v36, 0xbe0293ee, v36\n"
    "v_permlane32_swap_b32 v37, v39\n"
    "v_max_f32 v37, v37, v39\n"
    "v_mul_f32 v37, 0xbe0293ee, v37\n"
    "buffer_load_dwordx4 a[28:31], v0, s[40:43], s64 offen offset:3072 sc1\n"
    "v_fmamk_f32 v16, v16, 0x3e0293ee, v36\n"
    "v_fmamk_f32 v17, v17, 0x3e0293ee, v36\n"
    "v_exp_f32 v16, v16\n"
    "v_fmamk_f32 v18, v18, 0x3e0293ee, v36\n"
    "v_exp_f32 v17, v17\n"
    "v_fmamk_f32 v19, v19, 0x3e0293ee, v36\n"
    "v_exp_f32 v18, v18\n"
    "s_add_i32 s64, s65, 16384\n"
    "buffer_load_dwordx4 a[32:35], v0, s[40:43], s64 offen offset:0 sc1\n"
    "v_fmamk_f32 v28, v28, 0x3e0293ee, v36\n"
    "v_exp_f32 v19, v19\n"
    "v_fmamk_f32 v29, v29, 0x3e0293ee, v36\n"
    "v_exp_f32 v28, v28\n"
    "v_fmamk_f32 v30, v30, 0x3e0293ee, v36\n"
    "v_exp_f32 v29, v29\n"
    "v_fmamk_f32 v31, v31, 0x3e0293ee, v36\n"
    "v_exp_f32 v30, v30\n"
    "buffer_load_dwordx4 a[36:39], v0, s[40:43], s64 offen offset:1024 sc1\n"
    "v_fmamk_f32 v20, v20, 0x3e0293ee, v37\n"
    "v_exp_f32 v31, v31\n"
    "v_fmamk_f32 v21, v21, 0x3e0293ee, v37\n"
    "v_exp_f32 v20, v20\n"
    "v_fmamk_f32 v22, v22, 0x3e0293ee, v37\n"
    "v_exp_f32 v21, v21\n"
    "v_fmamk_f32 v23, v23, 0x3e0293ee, v37\n"
    "v_exp_f32 v22, v22\n"
    "buffer_load_dwordx4 a[40:43], v0, s[40:43], s64 offen offset:2048 sc1\n"
    "v_fmamk_f32 v24, v24, 0x3e0293ee, v37\n"
    "v_exp_f32 v23, v23\n"
    "v_fmamk_f32 v25, v25, 0x3e0293ee, v37\n"
    "v_exp_f32 v24, v24\n"
    "v_fmamk_f32 v26, v26, 0x3e0293ee, v37\n"
    "v_exp_f32 v25, v25\n"
    "v_fmamk_f32 v27, v27, 0x3e0293ee, v37\n"
    "v_exp_f32 v26, v26\n"
    "buffer_load_dwordx4 a[44:47], v0, s[40:43], s64 offen offset:3072 sc1\n"
    "v_fmamk_f32 v32, v32, 0x3e0293ee, v37\n"
    "v_exp_f32 v27, v27\n"
    "v_fmamk_f32 v33, v33, 0x3e0293ee, v37\n"
    "v_exp_f32 v32, v32\n"
    "v_fmamk_f32 v34, v34, 0x3e0293ee, v37\n"
    "v_exp_f32 v33, v33\n"
    "v_fmamk_f32 v35, v35, 0x3e0293ee, v37\n"
    "v_exp_f32 v34, v34\n"
    "s_add_i32 s64, s65, 20480\n"
    "buffer_load_dwordx4 a[48:51], v0, s[40:43], s64 offen offset:0 sc1\n"
    "v_exp_f32 v35, v35\n"
    "v_pk_add_f32 v[40:41], v[16:17], v[18:19]\n"
    "v_pk_add_f32 v[42:43], v[28:29], v[30:31]\n"
    "v_pk_add_f32 v[38:39], v[20:21], v[22:23]\n"
    "v_pk_add_f32 v[36:37], v[24:25], v[26:27]\n"
    "v_add_f32 v40, v40, v41\n"
    "v_add_f32 v42, v42, v43\n"
    "v_pk_add_f32 v[38:39], v[38:39], v[36:37]\n"
    "v_pk_add_f32 v[36:37], v[32:33], v[34:35]\n"
    "v_cndmask_b32 v42, 0, v42, s[60:61]\n"
    "v_add_f32 v41, v38, v39\n"
    "v_add_f32 v43, v36, v37\n"
    "v_add_f32 v40, v40, v42\n"
    "v_cndmask_b32 v43, 0, v43, s[60:61]\n"
    "v_mov_b32 v38, v40\n"
    "v_add_f32 v41, v41, v43\n"
    "v_mov_b32 v39, v41\n"
    "v_permlane16_swap_b32 v40, v38\n"
    "v_add_f32 v40, v40, v38\n"
    "v_mov_b32 v38, v40\n"
    "v_permlane16_swap_b32 v41, v39\n"
    "v_add_f32 v41, v41, v39\n"
    "v_mov_b32 v39, v41\n"
    "v_permlane32_swap_b32 v40, v38\n"
    "v_add_f32 v40, v40, v38\n"
    "v_rcp_f32 v40, v40\n"
    "v_permlane32_swap_b32 v41, v39\n"
    "v_add_f32 v42, v41, v39\n"
    "v_rcp_f32 v42, v42\n"
    "buffer_load_dwordx4 a[52:55], v0, s[40:43], s64 offen offset:1024 sc1\n"
    "v_pk_mul_f32 v[16:17], v[16:17], v[40:41] op_sel_hi:[1,0]\n"
    "v_pk_mul_f32 v[18:19], v[18:19], v[40:41] op_sel_hi:[1,0]\n"
    "v_pk_mul_f32 v[28:29], v[28:29], v[40:41] op_sel_hi:[1,0]\n"
    "v_pk_mul_f32 v[30:31], v[30:31], v[40:41] op_sel_hi:[1,0]\n"
    "v_cvt_pk_bf16_f32 v44, v16, v17\n"
    "v_cvt_pk_bf16_f32 v45, v18, v19\n"
    "v_cvt_pk_bf16_f32 v52, v28, v29\n"
    "v_cvt_pk_bf16_f32 v53, v30, v31\n"
    "s_waitcnt vmcnt(14)\n"
    "v_pk_mul_f32 v[20:21], v[20:21], v[42:43] op_sel_hi:[1,0]\n"
    "v_pk_mul_f32 v[22:23], v[22:23], v[42:43] op_sel_hi:[1,0]\n"
    "v_pk_mul_f32 v[24:25], v[24:25], v[42:43] op_sel_hi:[1,0]\n"
    "v_pk_mul_f32 v[26:27], v[26:27], v[42:43] op_sel_hi:[1,0]\n"
    "v_pk_mul_f32 v[32:33], v[32:33], v[42:43] op_sel_hi:[1,0]\n"
    "v_pk_mul_f32 v[34:35], v[34:35], v[42:43] op_sel_hi:[1,0]\n"
    "s_add_i32 s63, s71, 0x80000\n"
    "v_mfma_f32_16x16x32_bf16 v[56:59], a[160:163], v[44:47], 0\n"
    "v_cvt_pk_bf16_f32 v48, v20, v21\n"
    "v_cvt_pk_bf16_f32 v49, v22, v23\n"
    "v_mfma_f32_16x16x32_bf16 v[60:63], a[164:167], v[44:47], 0\n"
    "v_cvt_pk_bf16_f32 v50, v24, v25\n"
    "v_cvt_pk_bf16_f32 v51, v26, v27\n"
    "v_mfma_f32_16x16x16_bf16 v[56:59], a[208:209], v[52:53], v[56:59]\n"
    "v_cvt_pk_bf16_f32 v54, v32, v33\n"
    "v_cvt_pk_bf16_f32 v55, v34, v35\n"
    "v_mfma_f32_16x16x16_bf16 v[60:63], a[210:211], v[52:53], v[60:63]\n"
    "v_mfma_f32_16x16x32_bf16 v[72:75], a[168:171], v[44:47], 0\n"
    "v_mfma_f32_16x16x32_bf16 v[76:79], a[172:175], v[44:47], 0\n"
    "v_cvt_pk_bf16_f32 v88, v56, v57\n"
    "v_cvt_pk_bf16_f32 v89, v58, v59\n"
    "v_mfma_f32_16x16x16_bf16 v[72:75], a[212:213], v[52:53], v[72:75]\n"
    "v_cvt_pk_bf16_f32 v90, v60, v61\n"
    "v_cvt_pk_bf16_f32 v91, v62, v63\n"
    "v_mfma_f32_16x16x16_bf16 v[76:79], a[214:215], v[52:53], v[76:79]\n"
    "buffer_store_dwordx4 v[88:91], v0, s[36:39], s71 offen offset:0 sc1\n"
    "v_mfma_f32_16x16x32_bf16 v[64:67], a[160:163], v[48:51], 0\n"
    "v_mfma_f32_16x16x32_bf16 v[68:71], a[164:167], v[48:51], 0\n"
    "v_cvt_pk_bf16_f32 v96, v72, v73\n"
    "v_cvt_pk_bf16_f32 v97, v74, v75\n"
    "v_mfma_f32_16x16x16_bf16 v[64:67], a[208:209], v[54:55], v[64:67]\n"
    "v_cvt_pk_bf16_f32 v98, v76, v77\n"
    "v_cvt_pk_bf16_f32 v99, v78, v79\n"
    "v_mfma_f32_16x16x16_bf16 v[68:71], a[210:211], v[54:55], v[68:71]\n"
    "buffer_store_dwordx4 v[96:99], v0, s[36:39], s71 offen offset:1024 sc1\n"
    "v_mfma_f32_16x16x32_bf16 v[80:83], a[168:171], v[48:51], 0\n"
    "v_mfma_f32_16x16x32_bf16 v[84:87], a[172:175], v[48:51], 0\n"
    "v_cvt_pk_bf16_f32 v92, v64, v65\n"
    "v_cvt_pk_bf16_f32 v93, v66, v67\n"
    "v_mfma_f32_16x16x16_bf16 v[80:83], a[212:213], v[54:55], v[80:83]\n"
    "v_cvt_pk_bf16_f32 v94, v68, v69\n"
    "v_cvt_pk_bf16_f32 v95, v70, v71\n"
    "v_mfma_f32_16x16x16_bf16 v[84:87], a[214:215], v[54:55], v[84:87]\n"
    "buffer_store_dwordx4 v[92:95], v0, s[36:39], s63 offen offset:0 sc1\n"
    "v_mfma_f32_16x16x32_bf16 v[56:59], a[176:179], v[44:47], 0\n"
    "v_mfma_f32_16x16x32_bf16 v[60:63], a[180:183], v[44:47], 0\n"
    "v_cvt_pk_bf16_f32 v100, v80, v81\n"
    "v_cvt_pk_bf16_f32 v101, v82, v83\n"
    "v_mfma_f32_16x16x16_bf16 v[56:59], a[216:217], v[52:53], v[56:59]\n"
    "v_cvt_pk_bf16_f32 v102, v84, v85\n"
    "v_cvt_pk_bf16_f32 v103, v86, v87\n"
    "v_mfma_f32_16x16x16_bf16 v[60:63], a[218:219], v[52:53], v[60:63]\n"
    "buffer_store_dwordx4 v[100:103], v0, s[36:39], s63 offen offset:1024 sc1\n"
    "v_mfma_f32_16x16x32_bf16 v[72:75], a[184:187], v[44:47], 0\n"
    "v_mfma_f32_16x16x32_bf16 v[76:79], a[188:191], v[44:47], 0\n"
    "v_cvt_pk_bf16_f32 v88, v56, v57\n"
    "v_cvt_pk_bf16_f32 v89, v58, v59\n"
    "v_mfma_f32_16x16x16_bf16 v[72:75], a[220:221], v[52:53], v[72:75]\n"
    "v_cvt_pk_bf16_f32 v90, v60, v61\n"
    "v_cvt_pk_bf16_f32 v91, v62, v63\n"
    "v_mfma_f32_16x16x16_bf16 v[76:79], a[222:223], v[52:53], v[76:79]\n"
    "buffer_store_dwordx4 v[88:91], v0, s[36:39], s71 offen offset:2048 sc1\n"
    "v_mfma_f32_16x16x32_bf16 v[64:67], a[176:179], v[48:51], 0\n"
    "v_mfma_f32_16x16x32_bf16 v[68:71], a[180:183], v[48:51], 0\n"
    "v_cvt_pk_bf16_f32 v96, v72, v73\n"
    "v_cvt_pk_bf16_f32 v97, v74, v75\n"
    "v_mfma_f32_16x16x16_bf16 v[64:67], a[216:217], v[54:55], v[64:67]\n"
    "v_cvt_pk_bf16_f32 v98, v76, v77\n"
    "v_cvt_pk_bf16_f32 v99, v78, v79\n"
    "v_mfma_f32_16x16x16_bf16 v[68:71], a[218:219], v[54:55], v[68:71]\n"
    "buffer_store_dwordx4 v[96:99], v0, s[36:39], s71 offen offset:3072 sc1\n"
    "v_mfma_f32_16x16x32_bf16 v[80:83], a[184:187], v[48:51], 0\n"
    "v_mfma_f32_16x16x32_bf16 v[84:87], a[188:191], v[48:51], 0\n"
    "v_cvt_pk_bf16_f32 v92, v64, v65\n"
    "v_cvt_pk_bf16_f32 v93, v66, v67\n"
    "v_mfma_f32_16x16x16_bf16 v[80:83], a[220:221], v[54:55], v[80:83]\n"
    "v_cvt_pk_bf16_f32 v94, v68, v69\n"
    "v_cvt_pk_bf16_f32 v95, v70, v71\n"
    "v_mfma_f32_16x16x16_bf16 v[84:87], a[222:223], v[54:55], v[84:87]\n"
    "buffer_store_dwordx4 v[92:95], v0, s[36:39], s63 offen offset:2048 sc1\n"
    "s_nop 2\n"
    "v_cvt_pk_bf16_f32 v100, v80, v81\n"
    "v_cvt_pk_bf16_f32 v101, v82, v83\n"
    "s_nop 1\n"
    "v_cvt_pk_bf16_f32 v102, v84, v85\n"
    "v_cvt_pk_bf16_f32 v103, v86, v87\n"
    "buffer_store_dwordx4 v[100:103], v0, s[36:39], s63 offen offset:3072 sc1\n"
    "s_barrier\n"
    "buffer_load_dwordx4 a[56:59], v0, s[40:43], s64 offen offset:2048 sc1\n"
    "buffer_load_dwordx4 a[60:63], v0, s[40:43], s64 offen offset:3072 sc1\n"
    "s_add_i32 s64, s65, 32768\n"
    "buffer_load_dwordx4 a[64:67], v0, s[40:43], s64 offen offset:0 sc1\n"
    "buffer_load_dwordx4 a[68:71], v0, s[40:43], s64 offen offset:1024 sc1\n"
    "buffer_load_dwordx4 a[72:75], v0, s[40:43], s64 offen offset:2048 sc1\n"
    "buffer_load_dwordx4 a[76:79], v0, s[40:43], s64 offen offset:3072 sc1\n"
    "s_add_i32 s64, s65, 36864\n"
    "buffer_load_dwordx4 a[80:83], v0, s[40:43], s64 offen offset:0 sc1\n"
    "buffer_load_dwordx4 a[84:87], v0, s[40:43], s64 offen offset:1024 sc1\n"
    "buffer_load_dwordx4 a[88:91], v0, s[40:43], s64 offen offset:2048 sc1\n"
    "buffer_load_dwordx4 a[92:95], v0, s[40:43], s64 offen offset:3072 sc1\n"
    "s_waitcnt vmcnt(10)\n"
    "s_barrier\n"
    "v_mov_b32 v5, s62\n"
    "s_mov_b64 exec, s[68:69]\n"
    "buffer_store_dword v5, off, s[36:39], s49 sc1\n"
    "s_mov_b64 exec, -1\n"
    "s_add_i32 s62, s62, 1\n"
    "s_cmp_lt_u32 s62, 64\n"
    "s_cbranch_scc1 L_job0%=\n"
    "s_waitcnt vmcnt(0)\n"
    : "+{s[44:51]}"(s1)
      : "{s[36:39]}"(s0), "{s[52:59]}"(mk8), "{v[0:3]}"(c0), "{s80}"(node)
    : "v4", "v5", "v6", "v7", "v16", "v17", "v18", "v19", "v20", "v21", "v22", "v23", "v24", "v25", "v26", "v27", "v28", "v29", "v30", "v31",
      "v32", "v33", "v34", "v35", "v36", "v37", "v38", "v39", "v40", "v41", "v42", "v43", "v44", "v45", "v46", "v47", "v48", "v49", "v50", "v51",
      "v52", "v53", "v54", "v55", "v56", "v57", "v58", "v59", "v60", "v61", "v62", "v63", "v64", "v65", "v66", "v67", "v68", "v69", "v70", "v71",
      "v72", "v73", "v74", "v75", "v76", "v77", "v78", "v79", "v80", "v81", "v82", "v83", "v84", "v85", "v86", "v87", "v88", "v89", "v90", "v91",
      "v92", "v93", "v94", "v95", "v96", "v97", "v98", "v99", "v100", "v101", "v102", "v103", "a0", "a1", "a2", "a3", "a4", "a5", "a6", "a7",
      "a8", "a9", "a10", "a11", "a12", "a13", "a14", "a15", "a16", "a17", "a18", "a19", "a20", "a21", "a22", "a23", "a24", "a25", "a26", "a27",
      "a28", "a29", "a30", "a31", "a32", "a33", "a34", "a35", "a36", "a37", "a38", "a39", "a40", "a41", "a42", "a43", "a44", "a45", "a46", "a47",
      "a48", "a49", "a50", "a51", "a52", "a53", "a54", "a55", "a56", "a57", "a58", "a59", "a60", "a61", "a62", "a63", "a64", "a65", "a66", "a67",
      "a68", "a69", "a70", "a71", "a72", "a73", "a74", "a75", "a76", "a77", "a78", "a79", "a80", "a81", "a82", "a83", "a84", "a85", "a86", "a87",
      "a88", "a89", "a90", "a91", "a92", "a93", "a94", "a95", "a96", "a97", "a98", "a99", "a100", "a101", "a102", "a103", "a104", "a105", "a106", "a107",
      "a108", "a109", "a110", "a111", "a112", "a113", "a114", "a115", "a116", "a117", "a118", "a119", "a120", "a121", "a122", "a123", "a124", "a125", "a126", "a127",
      "a128", "a129", "a130", "a131", "a132", "a133", "a134", "a135", "a136", "a137", "a138", "a139", "a140", "a141", "a142", "a143", "a144", "a145", "a146", "a147",
      "a148", "a149", "a150", "a151", "a152", "a153", "a154", "a155", "a156", "a157", "a158", "a159", "a160", "a161", "a162", "a163", "a164", "a165", "a166", "a167",
      "a168", "a169", "a170", "a171", "a172", "a173", "a174", "a175", "a176", "a177", "a178", "a179", "a180", "a181", "a182", "a183", "a184", "a185", "a186", "a187",
      "a188", "a189", "a190", "a191", "a192", "a193", "a194", "a195", "a196", "a197", "a198", "a199", "a200", "a201", "a202", "a203", "a204", "a205", "a206", "a207",
      "a208", "a209", "a210", "a211", "a212", "a213", "a214", "a215", "a216", "a217", "a218", "a219", "a220", "a221", "a222", "a223", "s40", "s41", "s42", "s43",
      "s60", "s61", "s62", "s63", "s64", "s65", "s66", "s67", "s68", "s69", "s70", "s71", "vcc", "scc", "memory");
  __builtin_amdgcn_endpgm();
}

DI void chain_role(const Params& p, Ctx& c, LAS char* L, int node) {
  const int lane = c.lane, w = c.w, cl = node >> 5, h = node & 31;
  const int g = lane >> 4, b = (lane >> 3) & 1, r = lane & 7;
  v32u wa[8], wb[4]; v16u wc;
  LAS char* WK = L + w * 36864;
  LAS char* KR = L + 147456 + w * 4096;
  const unsigned wos = OFF_WOS + (unsigned)node * 32768u + (unsigned)w * 8192u;
  if (cl) nops(TV(5, DLY_CH));
  {
    rsrc_t rq = mk(p.wq, 576u * 8192u), rk = mk(p.wk, 576u * 8192u), rv = mk(p.wv, 576u * 8192u), ro = mk(p.wo, 4096u * 1152u);
    int rr = lane >> 2;
    unsigned v2 = (unsigned)((8 * (rr & 1) + 2 * ((rr >> 1) & 3) + (rr >> 3)) * 8192) + pair_chunk(lane);
    unsigned cb = (unsigned)(h * 128 + 32 * w) * 2u;
    int kk = piece_k(lane), ii = (kk & 15) >> 1;
    unsigned o1 = (unsigned)((4 * (kk >> 4) + (ii & 3) + 16 * (ii >> 2)) * 1152 + (kk & 1) * 32 + (lane & 1) * 16);
    v4u xo[12];
    auto reg = [&](auto nc, v4u f) { constexpr int n = decltype(nc)::v;
      if constexpr (n < 64) put4<(n & 7) * 4>(wa[n >> 3], f); else if constexpr (n < 96) put4<(n & 7) * 4>(wb[(n - 64) >> 3], f); else put4<(n & 3) * 4>(wc, f); };
    load_frags<112, 16>(WK, lane, [](auto ic) { constexpr int i = decltype(ic)::v; return IC<(i < 72 ? (i & 1) : 2)>{}; },
      [&](auto ic, LAS char* d) { constexpr int i = decltype(ic)::v;
        if constexpr (i < 36) dmaw(rq, v2, cb + i * 16 * 8192, d);
        else if constexpr (i < 72) dmaw(rv, v2, cb + (i - 36) * 16 * 8192, d);
        else { constexpr int q = i - 72, j = q >> 3, u = (q >> 1) & 3, hf = q & 1;
          dmaw(ro, o1, (unsigned)(h * 128 + 32 * ((w + u) & 3) + 8 * hf) * 1152u + (unsigned)(j < 4 ? 4 * w + j : 16 + (w >> 1)) * 64u, d); } },
      [&](auto ic, v4u f) { constexpr int i = decltype(ic)::v;
        if constexpr (i < 36) reg(IC<((i & 1) * 9 + (i >> 2)) * 2 + ((i >> 1) & 1)>{}, f);
        else if constexpr (i < 72) { constexpr int q = i - 36; reg(IC<36 + ((q >> 2) * 2 + (q & 1)) * 2 + ((q >> 1) & 1)>{}, f); }
        else if constexpr (i < 100) reg(IC<i>{}, f);
        else xo[i - 100] = f; });
    sfor<0, 36>([&](auto ic) { constexpr int i = decltype(ic)::v; dmaw(rk, v2, cb + i * 16 * 8192, WK + i * 1024); });
    WAITV(0);
    for (int f = 0; f < 9; ++f) {
      LAS char* q = WK + f * 4096;
      v4u f0 = tr_frag(q + tr_off2(lane)), f1 = tr_frag(q + 2048 + tr_off2(lane)), f2 = tr_frag(q + (tr_off2(lane) ^ 32u)), f3 = tr_frag(q + 2048 + (tr_off2(lane) ^ 32u));
      WAITL();
      if (f >= 2) { *(LAS v4u*)(q + lane * 16) = f0; *(LAS v4u*)(q + 1024 + lane * 16) = f1; *(LAS v4u*)(q + 2048 + lane * 16) = f2; *(LAS v4u*)(q + 3072 + lane * 16) = f3; }
      else { unsigned o = wos + (unsigned)f * 4096u, lo = (unsigned)lane * 16u;
        __builtin_amdgcn_raw_buffer_store_b128(f0, c.ws, lo, o, 0); __builtin_amdgcn_raw_buffer_store_b128(f1, c.ws, lo + 1024, o, 0);
        __builtin_amdgcn_raw_buffer_store_b128(f2, c.ws, lo + 2048, o, 0); __builtin_amdgcn_raw_buffer_store_b128(f3, c.ws, lo + 3072, o, 0);
        asm volatile("s_nop 1" :: "v"(f0), "v"(f1), "v"(f2), "v"(f3) : "memory"); }
    }
    WAITL(); BAR();
    #pragma unroll
    for (int i = 0; i < 12; ++i) if (i < 4 || !(w & 1)) *(LAS v4u*)(WK + i * 1024 + (i >= 8 ? 32768 : 0) + lane * 16) = xo[i];
    WAITL();
  }
  STAMPT(node, 128);
  get_par(c);
  wait_flags(c, flagoff(c, N_AT + cl * 8 + (lane & 7), 1), 0);
  const unsigned P0 = near(c, OFF_MSG + (unsigned)cl * PG, DIE_CH(cl)), P1 = P0 + 1179648u;
  const int task = w == 3 ? h : (w == 1 ? 32 + h : ((w == 2 && (h & 3) == 0) ? 64 + (h >> 2) : 72));
  const bool hasT = task < 72;
  const int tT = hasT ? task >> 1 : 0, tq = task & 1;
  const unsigned t_in = P0 + (unsigned)tT * 16384u + (unsigned)tq * 4096u;
  const unsigned t_out = P1 + (unsigned)((tT >> 2) < 8 ? (tT >> 2) * 1024 : 16384) + (unsigned)((tT & 3) * 256 + tq * 128);
  const unsigned lv = (unsigned)lane * 16u;
  const unsigned fq = flagoff(c, N_QKV + 2 * h + (lane & 1), cl);
  const unsigned kvb = head_base(c, cl, h) + 16384u + (unsigned)w * 2048u;
  wait_flags(c, fq, 0);
  dma(c.ws, lv, kvb, KR); dma(c.ws, lv, kvb + 1024, KR + 1024); dma(c.ws, lv, kvb + 16384, KR + 2048); dma(c.ws, lv, kvb + 17408, KR + 3072);
  const unsigned lb = (unsigned)(size_t)L, xb = lb + 147456u;
  v8u c0, c1; v4u c2, c3; unsigned c4; v16u s0, s1; v8u s2;
  c0[0] = lv; c0[1] = (unsigned)(g * 256 + (r >> 2) * 128 + b * 64 + (r & 3) * 16);
  c0[2] = (unsigned)((g >> 1) * 16384 + (g & 1) * 4096 + h * 128 + (lane & 15) * 8);
  c0[3] = lane < 32 ? P1 + 18432u + (unsigned)lane * TABS : (lane < 48 ? fq : flagoff(c, N_AT + cl * 8 + (h >> 2), 0)); c0[4] = b ? 0xDDDDu : 0x8888u; c0[5] = lb + (unsigned)w * 36864u + lv; c0[6] = xb + (unsigned)w * 4096u + lv;
  c0[7] = xb + (unsigned)((w + 1) & 3) * 4096u + lv; c2[0] = xb + (unsigned)((w + 2) & 3) * 4096u + lv; c2[1] = xb + (unsigned)((w + 3) & 3) * 4096u + lv;
  c2[2] = xb + (unsigned)w * 4096u + 1024u + (unsigned)lane * 8u; c2[3] = xb + 1024u + (unsigned)(((2 + (g & 1)) * 16 + (lane & 15)) * 8);
  c4 = b == 0 ? (unsigned)((g >> 1) * 32768 + (g & 1) * 64 + r * 8) : OOB;
  { int cc = lane & 15;
    #pragma unroll
    for (int d = 0; d < 4; ++d) {
      unsigned ut = 0;
      #pragma unroll
      for (int hh = 0; hh < 2; ++hh) { int j = 2 * d + hh;
        bool t1 = (j < 4) ? (cc < 8 && ((4 * g + j) & 7) == cc) : (cc >= 8 && ((4 * g + j - 4) & 7) == cc - 8);
        ut |= t1 ? (0x3F80u << (16 * hh)) : 0u; }
      c1[d] = ut; c1[4 + d] = 0xFFFFFFFFu; c3[d] = 0x3F803F80u;
    } }
  { unsigned long long wp = (unsigned long long)c.wsp; unsigned lo = (unsigned)wp, hi = (unsigned)(wp >> 32);
    unsigned long long mp = wp + P1 + 4096u, mp2 = wp + P1 + 16384u, wsq = wp + wos + 4096u, rp = wp + t_in;
    s0[0] = lo; s0[1] = hi & 0xFFFFu; s0[2] = WS_USED; s0[3] = 0x00020000u;
    s0[4] = lo; s0[5] = hi & 0xFFFFu; s0[6] = hasT ? WS_USED : 0u; s0[7] = 0x00020000u;
    s0[8] = P1 + 18432u + (unsigned)h * TABS; s0[9] = (unsigned)h; s0[10] = (0x4001u >> (w * 4 + (w >> 1))) & 3u;
    s0[11] = 0;
    s0[12] = (unsigned)mp; s0[13] = (unsigned)(mp >> 32); s0[14] = (unsigned)mp2; s0[15] = (unsigned)(mp2 >> 32);
    s1[0] = lo; s1[1] = hi; s1[2] = (unsigned)wsq; s1[3] = (unsigned)(wsq >> 32); s1[4] = (unsigned)rp; s1[5] = (unsigned)(rp >> 32);
    s1[6] = t_in; s1[7] = t_out; s1[8] = P0 + (unsigned)w * 131072u; s1[9] = P0 + (unsigned)(32 + 2 * (w >> 1)) * 16384u;
    s1[10] = kvm_base(c, cl, h) + (unsigned)w * 1024u; s1[11] = kvb; s1[12] = xb + (unsigned)w * 4096u;
    s1[13] = flagoff(c, node, 0); s1[14] = (unsigned)c.deadline; s1[15] = (unsigned)TV(0, D1V);
    s2[0] = (unsigned)TV(1, D2V); s2[1] = (unsigned)w; s2[2] = (unsigned)TV(6, D3V); s2[3] = (unsigned)TV(8, D4V); s2[4] = 0; s2[5] = 0; s2[6] = 0; s2[7] = 0; }
  uni<16>(s0); uni<16>(s1); uni<8>(s2);
  WAITV(0);
  asm volatile(
    "s_nop 4\n"
    "s_mov_b32 s4, s36\n"
    "s_mov_b32 s5, s37\n"
    "s_mov_b32 s7, s39\n"
    "s_memrealtime s[76:77]\n"
    "s_memtime s[78:79]\n"
    "s_mul_i32 s45, s67, 47\n"
    "s_lshr_b32 s45, s45, 10\n"
    "s_waitcnt lgkmcnt(0)\n"
    "s_mov_b32 s72, s76\n"
    "s_mov_b32 s73, s78\n"
    "s_sub_i32 s74, s68, 11\n"
    "s_mov_b32 s75, s62\n"
    "s_add_i32 s29, s44, 0x30000\n"
    "s_add_i32 s98, s59, 0x30000\n"
    "s_mov_b32 s0, 0\n"
    "s_mov_b32 s1, 0\n"
    "s_mov_b32 s30, 0x00ff00ff\n"
    "s_mov_b32 s31, 0x00ff00ff\n"
    "L_redo%=:\n"
    "global_load_dwordx4 v[80:83], v0, s[54:55] offset:-4096\n"
    "global_load_dwordx4 v[84:87], v0, s[54:55] offset:-3072\n"
    "global_load_dwordx4 v[88:91], v0, s[54:55] offset:-2048\n"
    "global_load_dwordx4 v[92:95], v0, s[54:55] offset:-1024\n"
    "global_load_dwordx4 v[96:99], v0, s[54:55] offset:0\n"
    "global_load_dwordx4 v[100:103], v0, s[54:55] offset:1024\n"
    "global_load_dwordx4 v[104:107], v0, s[54:55] offset:2048\n"
    "global_load_dwordx4 v[108:111], v0, s[54:55] offset:3072\n"
    "L_step%=:\n"
    "global_load_dwordx4 v[28:31], v1, s[48:49] offset:-4096 sc1\n"
    "v_mov_b64 v[64:65], 0\n"
    "s_nop 10\n"
    "global_load_dwordx4 v[32:35], v1, s[48:49] offset:-3072 sc1\n"
    "v_mov_b64 v[66:67], 0\n"
    "s_nop 10\n"
    "global_load_dwordx4 v[36:39], v1, s[48:49] offset:-2048 sc1\n"
    "v_mov_b64 v[68:69], 0\n"
    "s_nop 10\n"
    "global_load_dwordx4 v[40:43], v1, s[48:49] offset:-1024 sc1\n"
    "v_mov_b64 v[70:71], 0\n"
    "s_nop 10\n"
    "global_load_dwordx4 v[44:47], v1, s[48:49] offset:0 sc1\n"
    "v_mov_b64 v[72:73], 0\n"
    "s_nop 10\n"
    "global_load_dwordx4 v[48:51], v1, s[48:49] offset:1024 sc1\n"
    "v_mov_b64 v[74:75], 0\n"
    "s_nop 10\n"
    "global_load_dwordx4 v[52:55], v1, s[48:49] offset:2048 sc1\n"
    "v_mov_b64 v[76:77], 0\n"
    "s_nop 10\n"
    "global_load_dwordx4 v[56:59], v1, s[48:49] offset:3072 sc1\n"
    "v_mov_b64 v[78:79], 0\n"
    "s_nop 10\n"
    "global_load_dwordx4 v[60:63], v1, s[50:51] offset:0 sc1\n"
    "s_xor_b32 s3, s1, 0x90000\n"
    "s_add_i32 s3, s3, s58\n"
    "s_waitcnt vmcnt(8)\n"
    "v_cmp_eq_u32 vcc, -1, v31\n"
    "s_cbranch_vccnz L_fix0%=\n"
    "L_bk0%=:\n"
    "v_smfmac_f32_16x16x64_bf16 v[68:71], v[28:31], v[80:87], v4\n"
    "ds_read_b128 v[80:83], v5 offset:8192\n"
    "v_smfmac_f32_16x16x64_bf16 v[64:67], v[28:31], a[0:7], v4\n"
    "s_waitcnt vmcnt(7) lgkmcnt(3)\n"
    "ds_read_b128 v[84:87], v5 offset:9216\n"
    "v_cmp_eq_u32 vcc, -1, v35\n"
    "v_smfmac_f32_16x16x64_bf16 v[76:79], v[28:31], v[88:95], v4\n"
    "ds_read_b128 v[88:91], v5 offset:10240\n"
    "v_smfmac_f32_16x16x64_bf16 v[72:75], v[28:31], a[72:79], v4\n"
    "s_cbranch_vccnz L_fix1%=\n"
    "L_bk1%=:\n"
    "ds_read_b128 v[92:95], v5 offset:11264\n"
    "v_smfmac_f32_16x16x64_bf16 v[68:71], v[32:35], v[96:103], v4\n"
    "ds_read_b128 v[96:99], v5 offset:12288\n"
    "v_smfmac_f32_16x16x64_bf16 v[64:67], v[32:35], a[8:15], v4\n"
    "s_waitcnt vmcnt(6) lgkmcnt(3)\n"
    "ds_read_b128 v[100:103], v5 offset:13312\n"
    "v_cmp_eq_u32 vcc, -1, v39\n"
    "v_smfmac_f32_16x16x64_bf16 v[76:79], v[32:35], v[104:111], v4\n"
    "ds_read_b128 v[104:107], v5 offset:14336\n"
    "v_smfmac_f32_16x16x64_bf16 v[72:75], v[32:35], a[80:87], v4\n"
    "s_cbranch_vccnz L_fix2%=\n"
    "L_bk2%=:\n"
    "ds_read_b128 v[108:111], v5 offset:15360\n"
    "v_smfmac_f32_16x16x64_bf16 v[68:71], v[36:39], v[80:87], v4\n"
    "ds_read_b128 v[80:83], v5 offset:16384\n"
    "v_smfmac_f32_16x16x64_bf16 v[64:67], v[36:39], a[16:23], v4\n"
    "s_waitcnt vmcnt(5) lgkmcnt(3)\n"
    "ds_read_b128 v[84:87], v5 offset:17408\n"
    "v_cmp_eq_u32 vcc, -1, v43\n"
    "v_smfmac_f32_16x16x64_bf16 v[76:79], v[36:39], v[88:95], v4\n"
    "ds_read_b128 v[88:91], v5 offset:18432\n"
    "v_smfmac_f32_16x16x64_bf16 v[72:75], v[36:39], a[88:95], v4\n"
    "s_cbranch_vccnz L_fix3%=\n"
    "L_bk3%=:\n"
    "ds_read_b128 v[92:95], v5 offset:19456\n"
    "v_smfmac_f32_16x16x64_bf16 v[68:71], v[40:43], v[96:103], v4\n"
    "ds_read_b128 v[96:99], v5 offset:20480\n"
    "v_smfmac_f32_16x16x64_bf16 v[64:67], v[40:43], a[24:31], v4\n"
    "s_waitcnt vmcnt(4) lgkmcnt(3)\n"
    "ds_read_b128 v[100:103], v5 offset:21504\n"
    "v_cmp_eq_u32 vcc, -1, v47\n"
    "v_smfmac_f32_16x16x64_bf16 v[76:79], v[40:43], v[104:111], v4\n"
    "ds_read_b128 v[104:107], v5 offset:22528\n"
    "v_smfmac_f32_16x16x64_bf16 v[72:75], v[40:43], a[96:103], v4\n"
    "s_cbranch_vccnz L_fix4%=\n"
    "L_bk4%=:\n"
    "ds_read_b128 v[108:111], v5 offset:23552\n"
    "v_smfmac_f32_16x16x64_bf16 v[68:71], v[44:47], v[80:87], v4\n"
    "ds_read_b128 v[80:83], v5 offset:24576\n"
    "v_smfmac_f32_16x16x64_bf16 v[64:67], v[44:47], a[32:39], v4\n"
    "s_waitcnt vmcnt(3) lgkmcnt(3)\n"
    "ds_read_b128 v[84:87], v5 offset:25600\n"
    "v_cmp_eq_u32 vcc, -1, v51\n"
    "v_smfmac_f32_16x16x64_bf16 v[76:79], v[44:47], v[88:95], v4\n"
    "ds_read_b128 v[88:91], v5 offset:26624\n"
    "buffer_store_dwordx4 v[20:23], v0, s[40:43], s3 offen offset:0 sc1\n"
    "v_smfmac_f32_16x16x64_bf16 v[72:75], v[44:47], a[104:111], v4\n"
    "s_cbranch_vccnz L_fix5%=\n"
    "L_bk5%=:\n"
    "ds_read_b128 v[92:95], v5 offset:27648\n"
    "v_smfmac_f32_16x16x64_bf16 v[68:71], v[48:51], v[96:103], v4\n"
    "ds_read_b128 v[96:99], v5 offset:28672\n"
    "v_smfmac_f32_16x16x64_bf16 v[64:67], v[48:51], a[40:47], v4\n"
    "s_waitcnt vmcnt(3) lgkmcnt(3)\n"
    "ds_read_b128 v[100:103], v5 offset:29696\n"
    "v_cmp_eq_u32 vcc, -1, v55\n"
    "v_smfmac_f32_16x16x64_bf16 v[76:79], v[48:51], v[104:111], v4\n"
    "ds_read_b128 v[104:107], v5 offset:30720\n"
    "buffer_store_dwordx4 v[20:23], v0, s[40:43], s3 offen offset:1024 sc1\n"
    "v_smfmac_f32_16x16x64_bf16 v[72:75], v[48:51], a[112:119], v4\n"
    "s_cbranch_vccnz L_fix6%=\n"
    "L_bk6%=:\n"
    "ds_read_b128 v[108:111], v5 offset:31744\n"
    "v_smfmac_f32_16x16x64_bf16 v[68:71], v[52:55], v[80:87], v4\n"
    "ds_read_b128 v[80:83], v5 offset:32768\n"
    "v_smfmac_f32_16x16x64_bf16 v[64:67], v[52:55], a[48:55], v4\n"
    "s_waitcnt vmcnt(3) lgkmcnt(3)\n"
    "ds_read_b128 v[84:87], v5 offset:33792\n"
    "v_cmp_eq_u32 vcc, -1, v59\n"
    "v_smfmac_f32_16x16x64_bf16 v[76:79], v[52:55], v[88:95], v4\n"
    "ds_read_b128 v[88:91], v5 offset:34816\n"
    "buffer_store_dwordx4 v[20:23], v0, s[40:43], s3 offen offset:2048 sc1\n"
    "v_smfmac_f32_16x16x64_bf16 v[72:75], v[52:55], a[120:127], v4\n"
    "s_cbranch_vccnz L_fix7%=\n"
    "L_bk7%=:\n"
    "ds_read_b128 v[92:95], v5 offset:35840\n"
    "v_smfmac_f32_16x16x64_bf16 v[68:71], v[56:59], v[96:103], v4\n"
    "ds_read_b128 v[96:99], v6 offset:0\n"
    "v_smfmac_f32_16x16x64_bf16 v[64:67], v[56:59], a[56:63], v4\n"
    "s_waitcnt vmcnt(3) lgkmcnt(3)\n"
    "ds_read_b128 v[100:103], v6 offset:1024\n"
    "v_cmp_eq_u32 vcc, -1, v63\n"
    "v_smfmac_f32_16x16x64_bf16 v[76:79], v[56:59], v[104:111], v4\n"
    "ds_read_b128 v[104:107], v6 offset:2048\n"
    "buffer_store_dwordx4 v[20:23], v0, s[40:43], s3 offen offset:3072 sc1\n"
    "v_smfmac_f32_16x16x64_bf16 v[72:75], v[56:59], a[128:135], v4\n"
    "s_cbranch_vccnz L_fix8%=\n"
    "L_bk8%=:\n"
    "ds_read_b128 v[108:111], v6 offset:3072\n"
    "v_smfmac_f32_16x16x64_bf16 v[68:71], v[60:63], v[80:87], v4\n"
    "s_mov_b32 exec_lo, 0\n"
    "global_load_dword v13, v3, s[52:53] sc1\n"
    "s_mov_b32 exec_lo, -1\n"
    "v_mov_b64 v[80:81], 0\n"
    "v_smfmac_f32_16x16x64_bf16 v[64:67], v[60:63], a[64:71], v4\n"
    "s_waitcnt lgkmcnt(4)\n"
    "v_mov_b64 v[82:83], 0\n"
    "v_smfmac_f32_16x16x64_bf16 v[76:79], v[60:63], v[88:95], v4\n"
    "buffer_store_dwordx2 v[20:21], v12, s[40:43], s98 offen sc1\n"
    "s_memrealtime s[34:35]\n"
    "s_memtime s[96:97]\n"
    "v_mov_b64 v[84:85], 0\n"
    "v_smfmac_f32_16x16x64_bf16 v[72:75], v[60:63], a[136:143], v4\n"
    "v_cvt_pk_bf16_f32 v64, v64, v65\n"
    "v_cvt_pk_bf16_f32 v65, v66, v67\n"
    "v_cvt_pk_bf16_f32 v66, v68, v69\n"
    "v_cvt_pk_bf16_f32 v67, v70, v71\n"
    "v_cvt_pk_bf16_f32 v90, v76, v77\n"
    "v_cvt_pk_bf16_f32 v91, v78, v79\n"
    "v_mfma_f32_16x16x32_bf16 v[68:71], v[64:67], v[16:19], 0\n"
    "v_cvt_pk_bf16_f32 v88, v72, v73\n"
    "v_cvt_pk_bf16_f32 v89, v74, v75\n"
    "v_mov_b64 v[86:87], 0\n"
    "s_nop 0\n"
    "v_mfma_f32_16x16x32_bf16 v[76:79], v[88:91], v[16:19], 0\n"
    "v_smfmac_f32_16x16x64_bf16 v[80:83], v[28:31], a[144:151], v4\n"
    "s_waitcnt lgkmcnt(0)\n"
    "s_add_i32 s2, s34, s45\n"
    "v_cvt_pk_bf16_f32 v64, v68, v69\n"
    "v_cvt_pk_bf16_f32 v65, v70, v71\n"
    "v_cvt_pk_bf16_f32 v66, v76, v77\n"
    "v_cvt_pk_bf16_f32 v67, v78, v79\n"
    "v_cmp_u_f32 vcc, v68, v68\n"
    "v_mov_b32 v92, s2\n"
    "v_mfma_f32_16x16x32_bf16 v[68:71], v[96:99], v[64:67], 0\n"
    "v_mfma_f32_16x16x32_bf16 v[72:75], v[100:103], v[64:67], 0\n"
    "s_cbranch_vccnz L_chkA%=\n"
    "L_okA%=:\n"
    "v_mfma_f32_16x16x32_bf16 v[76:79], v[64:67], v[64:67], 0\n"
    "s_nop 3\n"
    "v_mov_b32_dpp v68, v72 row_ror:8 row_mask:0xf bank_mask:0xc\n"
    "v_mov_b32_dpp v69, v73 row_ror:8 row_mask:0xf bank_mask:0xc\n"
    "v_mov_b32_dpp v70, v74 row_ror:8 row_mask:0xf bank_mask:0xc\n"
    "v_mov_b32_dpp v71, v75 row_ror:8 row_mask:0xf bank_mask:0xc\n"
    "v_mov_b32_dpp v76, v78 row_ror:8 row_mask:0xf bank_mask:0xc\n"
    "v_mov_b32_dpp v77, v79 row_ror:8 row_mask:0xf bank_mask:0xc\n"
    "ds_write_b64 v10, v[76:77]\n"
    "ds_write_b128 v6, v[68:71]\n"
    "v_smfmac_f32_16x16x64_bf16 v[84:87], v[28:31], a[152:159], v4\n"
    "s_lshr_b64 exec, s[46:47], 1\n"
    "buffer_store_dword v92, off, s[36:39], s44 sc1\n"
    "s_mov_b64 exec, -1\n"
    "v_smfmac_f32_16x16x64_bf16 v[80:83], v[32:35], a[160:167], v4\n"
    "v_smfmac_f32_16x16x64_bf16 v[84:87], v[32:35], a[168:175], v4\n"
    "v_smfmac_f32_16x16x64_bf16 v[80:83], v[36:39], a[176:183], v4\n"
    "v_smfmac_f32_16x16x64_bf16 v[84:87], v[36:39], a[184:191], v4\n"
    "v_smfmac_f32_16x16x64_bf16 v[80:83], v[40:43], a[192:199], v4\n"
    "v_smfmac_f32_16x16x64_bf16 v[84:87], v[40:43], a[200:207], v4\n"
    "v_smfmac_f32_16x16x64_bf16 v[80:83], v[44:47], a[208:215], v4\n"
    "s_waitcnt lgkmcnt(0)\n"
    "s_barrier\n"
    "ds_read_b128 v[72:75], v7 offset:0\n"
    "ds_read_b128 v[92:95], v8 offset:0\n"
    "ds_read_b128 v[96:99], v9 offset:0\n"
    "ds_read_b64 v[100:101], v11 offset:0\n"
    "ds_read_b64 v[102:103], v11 offset:4096\n"
    "ds_read_b64 v[76:77], v11 offset:8192\n"
    "ds_read_b64 v[78:79], v11 offset:12288\n"
    "v_smfmac_f32_16x16x64_bf16 v[84:87], v[44:47], a[216:223], v4\n"
    "ds_read_b128 v[28:31], v5 offset:0\n"
    "v_smfmac_f32_16x16x64_bf16 v[80:83], v[48:51], a[224:231], v4\n"
    "ds_read_b128 v[32:35], v5 offset:1024\n"
    "v_smfmac_f32_16x16x64_bf16 v[84:87], v[48:51], a[232:239], v4\n"
    "ds_read_b128 v[36:39], v5 offset:2048\n"
    "v_smfmac_f32_16x16x64_bf16 v[80:83], v[52:55], a[240:247], v4\n"
    "ds_read_b128 v[40:43], v5 offset:3072\n"
    "v_smfmac_f32_16x16x64_bf16 v[84:87], v[52:55], a[248:255], v4\n"
    "s_not_b64 exec, s[30:31]\n"
    "buffer_store_dwordx4 v[64:67], v0, s[36:39], s75 offen sc1\n"
    "s_mov_b64 exec, -1\n"
    "v_smfmac_f32_16x16x64_bf16 v[80:83], v[56:59], v[112:119], v4\n"
    "s_waitcnt lgkmcnt(10)\n"
    "v_pk_add_f32 v[68:69], v[68:69], v[72:73]\n"
    "v_pk_add_f32 v[70:71], v[70:71], v[74:75]\n"
    "s_waitcnt lgkmcnt(8)\n"
    "v_pk_add_f32 v[92:93], v[92:93], v[96:97]\n"
    "v_pk_add_f32 v[94:95], v[94:95], v[98:99]\n"
    "s_waitcnt lgkmcnt(4)\n"
    "v_pk_add_f32 v[100:101], v[100:101], v[102:103]\n"
    "v_pk_add_f32 v[76:77], v[76:77], v[78:79]\n"
    "v_pk_add_f32 v[68:69], v[68:69], v[92:93]\n"
    "v_pk_add_f32 v[70:71], v[70:71], v[94:95]\n"
    "v_pk_add_f32 v[72:73], v[100:101], v[76:77]\n"
    "v_max3_f32 v74, v68, v69, v70\n"
    "v_max3_f32 v75, v71, v72, v73\n"
    "v_max_f32 v74, v74, v75\n"
    "v_smfmac_f32_16x16x64_bf16 v[84:87], v[56:59], v[120:127], v4\n"
    "s_nop 0\n"
    "v_max_f32_dpp v74, v74, v74 row_ror:8 row_mask:0xf bank_mask:0xf\n"
    "v_mov_b32 v75, v74\n"
    "v_smfmac_f32_16x16x64_bf16 v[80:83], v[60:63], v[128:135], v4\n"
    "s_nop 0\n"
    "v_permlane16_swap_b32 v74, v75\n"
    "v_max_f32 v74, v74, v75\n"
    "v_mov_b32 v75, v74\n"
    "v_smfmac_f32_16x16x64_bf16 v[84:87], v[60:63], v[136:143], v4\n"
    "s_nop 0\n"
    "v_permlane32_swap_b32 v74, v75\n"
    "v_max_f32 v74, v74, v75\n"
    "v_mul_f32 v74, 0xbe0293ee, v74\n"
    "v_fmamk_f32 v68, v68, 0x3e0293ee, v74\n"
    "v_fmamk_f32 v69, v69, 0x3e0293ee, v74\n"
    "v_exp_f32 v68, v68\n"
    "v_fmamk_f32 v70, v70, 0x3e0293ee, v74\n"
    "v_exp_f32 v69, v69\n"
    "v_fmamk_f32 v71, v71, 0x3e0293ee, v74\n"
    "v_exp_f32 v70, v70\n"
    "v_fmamk_f32 v72, v72, 0x3e0293ee, v74\n"
    "v_exp_f32 v71, v71\n"
    "v_fmamk_f32 v73, v73, 0x3e0293ee, v74\n"
    "v_exp_f32 v72, v72\n"
    "s_mov_b64 vcc, s[30:31]\n"
    "v_exp_f32 v73, v73\n"
    "v_cvt_pk_bf16_f32 v74, v68, v69\n"
    "v_cvt_pk_bf16_f32 v75, v70, v71\n"
    "v_cvt_pk_bf16_f32 v76, v72, v73\n"
    "v_cvt_pk_bf16_f32 v80, v80, v81\n"
    "v_cndmask_b32_dpp v96, v74, v74, vcc row_ror:8 row_mask:0xf bank_mask:0xf\n"
    "v_cndmask_b32_dpp v97, v75, v75, vcc row_ror:8 row_mask:0xf bank_mask:0xf\n"
    "v_cndmask_b32_dpp v100, v76, v76, vcc row_ror:8 row_mask:0xf bank_mask:0xf\n"
    "s_not_b64 vcc, vcc\n"
    "v_cvt_pk_bf16_f32 v81, v82, v83\n"
    "v_cndmask_b32_dpp v98, v74, v74, vcc row_ror:8 row_mask:0xf bank_mask:0xf\n"
    "v_cndmask_b32_dpp v99, v75, v75, vcc row_ror:8 row_mask:0xf bank_mask:0xf\n"
    "v_cndmask_b32_dpp v101, v76, v76, vcc row_ror:8 row_mask:0xf bank_mask:0xf\n"
    "v_cvt_pk_bf16_f32 v82, v84, v85\n"
    "v_mfma_f32_16x16x32_bf16 v[68:71], v[104:107], v[96:99], 0\n"
    "v_cvt_pk_bf16_f32 v83, v86, v87\n"
    "v_mfma_f32_16x16x32_bf16 v[76:79], v[24:27], v[96:99], 0\n"
    "v_mfma_f32_16x16x16_bf16 v[84:87], v[24:25], v[100:101], 0\n"
    "v_mfma_f32_16x16x32_bf16 v[72:75], v[108:111], v[96:99], 0\n"
    "s_add_i32 s2, s75, 0x1000\n"
    "s_add_i32 s3, s60, s1\n"
    "v_mfma_f32_16x16x16_bf16 v[68:71], v[80:81], v[100:101], v[68:71]\n"
    "v_fmac_f32 v76, 0.5, v84\n"
    "v_mfma_f32_16x16x16_bf16 v[72:75], v[82:83], v[100:101], v[72:75]\n"
    "v_rcp_f32 v76, v76\n"
    "s_nop 0\n"
    "s_nop 0\n"
    "v_pk_mul_f32 v[68:69], v[68:69], v[76:77] op_sel_hi:[1,0]\n"
    "v_pk_mul_f32 v[70:71], v[70:71], v[76:77] op_sel_hi:[1,0]\n"
    "v_cvt_pk_bf16_f32 v96, v68, v69\n"
    "v_cvt_pk_bf16_f32 v97, v70, v71\n"
    "v_pk_mul_f32 v[72:73], v[72:73], v[76:77] op_sel_hi:[1,0]\n"
    "v_pk_mul_f32 v[74:75], v[74:75], v[76:77] op_sel_hi:[1,0]\n"
    "v_cvt_pk_bf16_f32 v98, v72, v73\n"
    "v_cvt_pk_bf16_f32 v99, v74, v75\n"
    "ds_write_b128 v6, v[96:99] offset:2048\n"
    "s_mov_b32 exec_hi, 0\n"
    "global_load_dword v13, v3, s[52:53] sc1\n"
    "s_mov_b32 exec_hi, -1\n"
    "s_cmp_ge_u32 s0, 63\n"
    "buffer_store_dwordx4 v[80:83], v0, s[36:39], s2 offen sc1\n"
    "s_cbranch_scc1 L_end%=\n"
    "v_mov_b64 v[68:69], 0\n"
    "v_mov_b64 v[70:71], 0\n"
    "v_mov_b64 v[72:73], 0\n"
    "v_mov_b64 v[74:75], 0\n"
    "v_mov_b64 v[76:77], 0\n"
    "v_mov_b64 v[78:79], 0\n"
    "v_mov_b64 v[80:81], 0\n"
    "v_mov_b64 v[82:83], 0\n"
    "v_mov_b64 v[84:85], 0\n"
    "v_mov_b64 v[86:87], 0\n"
    "s_waitcnt lgkmcnt(0)\n"
    "s_barrier\n"
    "ds_read_b128 v[100:103], v7 offset:2048\n"
    "ds_read_b128 v[104:107], v8 offset:2048\n"
    "ds_read_b128 v[108:111], v9 offset:2048\n"
    "v_smfmac_f32_16x16x64_bf16 v[68:71], v[96:99], v[144:151], v4\n"
    "ds_read_b128 v[44:47], v5 offset:4096\n"
    "v_smfmac_f32_16x16x64_bf16 v[72:75], v[96:99], v[176:183], v4\n"
    "ds_read_b128 v[48:51], v5 offset:5120\n"
    "v_smfmac_f32_16x16x64_bf16 v[76:79], v[96:99], v[208:215], v4\n"
    "ds_read_b128 v[52:55], v5 offset:6144\n"
    "v_smfmac_f32_16x16x64_bf16 v[80:83], v[96:99], v[240:247], v4\n"
    "s_waitcnt lgkmcnt(5)\n"
    "ds_read_b128 v[56:59], v5 offset:7168\n"
    "v_smfmac_f32_16x16x64_bf16 v[68:71], v[100:103], v[152:159], v4\n"
    "s_waitcnt lgkmcnt(4)\n"
    "ds_read_b128 v[60:63], v5 offset:40960\n"
    "v_smfmac_f32_16x16x64_bf16 v[68:71], v[104:107], v[160:167], v4\n"
    "ds_read_b128 v[64:67], v5 offset:41984\n"
    "v_smfmac_f32_16x16x64_bf16 v[68:71], v[108:111], v[168:175], v4\n"
    "ds_read_b128 v[88:91], v5 offset:43008\n"
    "v_smfmac_f32_16x16x64_bf16 v[72:75], v[100:103], v[184:191], v4\n"
    "ds_read_b128 v[92:95], v5 offset:44032\n"
    "v_smfmac_f32_16x16x64_bf16 v[72:75], v[104:107], v[192:199], v4\n"
    "v_smfmac_f32_16x16x64_bf16 v[72:75], v[108:111], v[200:207], v4\n"
    "v_cvt_pk_bf16_f32 v68, v68, v69\n"
    "v_smfmac_f32_16x16x64_bf16 v[76:79], v[100:103], v[216:223], v4\n"
    "v_cvt_pk_bf16_f32 v69, v70, v71\n"
    "v_smfmac_f32_16x16x64_bf16 v[76:79], v[104:107], v[224:231], v4\n"
    "buffer_store_dwordx2 v[68:69], v2, s[36:39], s3 offen sc1\n"
    "v_smfmac_f32_16x16x64_bf16 v[76:79], v[108:111], v[232:239], v4\n"
    "v_cvt_pk_bf16_f32 v72, v72, v73\n"
    "v_smfmac_f32_16x16x64_bf16 v[80:83], v[100:103], v[248:255], v4\n"
    "s_add_i32 s3, s3, 0x8000\n"
    "v_cvt_pk_bf16_f32 v73, v74, v75\n"
    "v_smfmac_f32_16x16x64_bf16 v[80:83], v[104:107], v[28:35], v4\n"
    "s_bitcmp0_b32 s69, 0\n"
    "buffer_store_dwordx2 v[72:73], v2, s[36:39], s3 offen sc1\n"
    "v_smfmac_f32_16x16x64_bf16 v[80:83], v[108:111], v[36:43], v4\n"
    "s_waitcnt lgkmcnt(0)\n"
    "s_cbranch_scc0 L_np4%=\n"
    "v_smfmac_f32_16x16x64_bf16 v[84:87], v[96:99], v[44:51], v4\n"
    "s_add_i32 s3, s3, 0x8000\n"
    "v_cvt_pk_bf16_f32 v76, v76, v77\n"
    "v_smfmac_f32_16x16x64_bf16 v[84:87], v[100:103], v[52:59], v4\n"
    "v_cvt_pk_bf16_f32 v77, v78, v79\n"
    "v_smfmac_f32_16x16x64_bf16 v[84:87], v[104:107], v[60:67], v4\n"
    "buffer_store_dwordx2 v[76:77], v2, s[36:39], s3 offen sc1\n"
    "v_cvt_pk_bf16_f32 v80, v80, v81\n"
    "v_smfmac_f32_16x16x64_bf16 v[84:87], v[108:111], v[88:95], v4\n"
    "s_add_i32 s3, s3, 0x8000\n"
    "v_cvt_pk_bf16_f32 v81, v82, v83\n"
    "buffer_store_dwordx2 v[80:81], v2, s[36:39], s3 offen sc1\n"
    "s_add_i32 s3, s61, s1\n"
    "s_nop 3\n"
    "v_cvt_pk_bf16_f32 v84, v84, v85\n"
    "v_cvt_pk_bf16_f32 v85, v86, v87\n"
    "buffer_store_dwordx2 v[84:85], v2, s[36:39], s3 offen sc1\n"
    "s_waitcnt vmcnt(6)\n"
    "s_branch L_p4d%=\n"
    "L_np4%=:\n"
    "v_cvt_pk_bf16_f32 v76, v76, v77\n"
    "v_cvt_pk_bf16_f32 v77, v78, v79\n"
    "s_add_i32 s3, s3, 0x8000\n"
    "buffer_store_dwordx2 v[76:77], v2, s[36:39], s3 offen sc1\n"
    "s_add_i32 s3, s3, 0x8000\n"
    "s_nop 1\n"
    "v_cvt_pk_bf16_f32 v80, v80, v81\n"
    "v_cvt_pk_bf16_f32 v81, v82, v83\n"
    "buffer_store_dwordx2 v[80:81], v2, s[36:39], s3 offen sc1\n"
    "s_waitcnt vmcnt(5)\n"
    "L_p4d%=:\n"
    "L_tab%=:\n"
    "v_cmp_eq_u32 vcc, -1, v13\n"
    "v_cmp_lt_i32 s[76:77], s0, v13\n"
    "s_sub_i32 s2, s0, 8\n"
    "v_cmp_lt_i32 s[98:99], s2, v13\n"
    "v_subrev_u32 v53, s34, v13\n"
    "v_sub_u32 v54, s34, v13\n"
    "s_nop 0\n"
    "v_max_i32_dpp v53, v53, v53 quad_perm:[1,0,3,2] row_mask:0xf bank_mask:0xf\n"
    "v_max_i32_dpp v54, v54, v54 quad_perm:[1,0,3,2] row_mask:0xf bank_mask:0xf\n"
    "s_nop 0\n"
    "v_max_i32_dpp v53, v53, v53 quad_perm:[2,3,0,1] row_mask:0xf bank_mask:0xf\n"
    "v_max_i32_dpp v54, v54, v54 quad_perm:[2,3,0,1] row_mask:0xf bank_mask:0xf\n"
    "s_nop 0\n"
    "v_max_i32_dpp v53, v53, v53 row_ror:4 row_mask:0xf bank_mask:0xf\n"
    "v_max_i32_dpp v54, v54, v54 row_ror:4 row_mask:0xf bank_mask:0xf\n"
    "s_nop 0\n"
    "v_max_i32_dpp v53, v53, v53 row_ror:8 row_mask:0xf bank_mask:0xf\n"
    "v_max_i32_dpp v54, v54, v54 row_ror:8 row_mask:0xf bank_mask:0xf\n"
    "s_cmp_lg_u32 vcc_lo, 0\n"
    "s_cbranch_scc1 L_tabw%=\n"
    "L_tabd%=:\n"
    "v_readlane_b32 s2, v53, 0\n"
    "v_readlane_b32 s3, v53, 16\n"
    "v_readlane_b32 s78, v54, 0\n"
    "v_readlane_b32 s79, v54, 16\n"
    "s_or_b32 s77, s77, 0xffff0000\n"
    "s_and_b32 s77, s77, s99\n"
    "s_cmp_eq_u32 s77, -1\n"
    "s_cselect_b32 s6, s38, 0\n"
    "s_max_i32 s2, s2, s3\n"
    "s_max_i32 s78, s78, s79\n"
    "s_min_i32 s2, s2, 2000\n"
    "s_max_i32 s2, s2, -2000\n"
    "s_add_i32 s78, s78, s2\n"
    "s_add_i32 s28, s2, s34\n"
    "s_cmp_gt_i32 s78, s70\n"
    "s_cselect_b32 s2, s71, 0\n"
    "s_add_i32 s28, s28, s2\n"
    "s_add_u32 s98, s56, s1\n"
    "s_addc_u32 s99, s57, 0\n"
    "s_cmp_eq_u32 s42, 0\n"
    "s_cbranch_scc1 L_notask%=\n"
    "s_bitcmp0_b32 s69, 0\n"
    "s_cbranch_scc1 L_nokb%=\n"
    "global_load_dwordx4 v[80:83], v0, s[54:55] offset:-4096\n"
    "global_load_dwordx4 v[84:87], v0, s[54:55] offset:-3072\n"
    "global_load_dwordx4 v[88:91], v0, s[54:55] offset:-2048\n"
    "global_load_dwordx4 v[92:95], v0, s[54:55] offset:-1024\n"
    "global_load_dwordx4 v[96:99], v0, s[54:55] offset:0\n"
    "L_nokb%=:\n"
    "s_add_i32 s3, s28, 0\n"
    "L_d1%=a:\n"
    "s_memrealtime s[78:79]\n"
    "s_waitcnt lgkmcnt(0)\n"
    "s_sub_i32 s2, s3, s78\n"
    "s_cmp_gt_i32 s2, 3\n"
    "s_cbranch_scc1 L_d1%=a\n"
    "L_d1%=:\n"
    "s_sub_i32 s2, s2, 1\n"
    "s_cmp_ge_i32 s2, 0\n"
    "s_cbranch_scc1 L_d1%=\n"
    "global_load_dwordx4 v[28:31], v0, s[98:99] offset:0 sc1\n"
    "global_load_dwordx4 v[32:35], v0, s[98:99] offset:1024 sc1\n"
    "global_load_dwordx4 v[36:39], v0, s[98:99] offset:2048 sc1\n"
    "global_load_dwordx4 v[40:43], v0, s[98:99] offset:3072 sc1\n"
    "s_add_i32 s2, s0, 1\n"
    "s_and_b32 s35, s2, 15\n"
    "s_mul_i32 s35, s35, 0xc000\n"
    "s_add_i32 s35, s35, s63\n"
    "s_and_b32 s75, s2, 7\n"
    "s_lshl_b32 s75, s75, 14\n"
    "s_add_i32 s75, s75, s62\n"
    "s_and_b32 s2, s2, 3\n"
    "s_mov_b32 s97, 0xfffd0000\n"
    "s_cmp_lg_u32 s2, 0\n"
    "s_cselect_b32 s97, 0x10000, s97\n"
    "s_cselect_b32 s2, 0, -1\n"
    "s_add_u32 s48, s48, s97\n"
    "s_addc_u32 s49, s49, s2\n"
    "s_add_u32 s50, s50, s97\n"
    "s_addc_u32 s51, s51, s2\n"
    "s_add_i32 s59, s59, s97\n"
    "s_waitcnt vmcnt(9)\n"
    "s_barrier\n"
    "s_add_i32 m0, s64, 0\n"
    "s_add_i32 s2, s35, 0\n"
    "buffer_load_dwordx4 v0, s[4:7], s2 offen sc1 lds\n"
    "s_add_i32 m0, s64, 1024\n"
    "s_add_i32 s2, s35, 1024\n"
    "buffer_load_dwordx4 v0, s[4:7], s2 offen sc1 lds\n"
    "s_add_i32 m0, s64, 2048\n"
    "s_add_i32 s2, s35, 16384\n"
    "buffer_load_dwordx4 v0, s[4:7], s2 offen sc1 lds\n"
    "s_add_i32 m0, s64, 3072\n"
    "s_add_i32 s2, s35, 17408\n"
    "buffer_load_dwordx4 v0, s[4:7], s2 offen sc1 lds\n"
    "v_lshrrev_b32 v48, 4, v0\n"
    "v_and_b32 v48, 15, v48\n"
    "v_bfe_u32 v53, v48, 1, 1\n"
    "v_lshlrev_b32 v53, 4, v53\n"
    "v_mov_b32 v49, 0x3f80\n"
    "v_lshlrev_b32 v53, v53, v49\n"
    "v_and_b32 v52, 13, v48\n"
    "v_cmp_eq_u32 vcc, 0, v52\n"
    "s_nop 1\n"
    "v_cndmask_b32 v48, 0, v53, vcc\n"
    "v_cmp_eq_u32 vcc, 4, v52\n"
    "s_nop 1\n"
    "v_cndmask_b32 v49, 0, v53, vcc\n"
    "v_cmp_eq_u32 vcc, 1, v52\n"
    "s_nop 1\n"
    "v_cndmask_b32 v50, 0, v53, vcc\n"
    "v_cmp_eq_u32 vcc, 5, v52\n"
    "s_nop 1\n"
    "v_cndmask_b32 v51, 0, v53, vcc\n"
    "s_bitcmp0_b32 s69, 0\n"
    "s_cbranch_scc1 L_evt%=\n"
    "s_waitcnt vmcnt(7)\n"
    "v_mfma_f32_16x16x32_bf16 v[44:47], v[28:31], v[48:51], 0\n"
    "s_waitcnt vmcnt(6)\n"
    "v_mfma_f32_16x16x32_bf16 v[44:47], v[32:35], v[48:51], v[44:47]\n"
    "s_waitcnt vmcnt(5)\n"
    "v_mfma_f32_16x16x32_bf16 v[44:47], v[36:39], v[48:51], v[44:47]\n"
    "s_waitcnt vmcnt(4)\n"
    "v_mfma_f32_16x16x32_bf16 v[44:47], v[40:43], v[48:51], v[44:47]\n"
    "L_red%=:\n"
    "s_nop 7\n"
    "v_permlane32_swap_b32 v44, v46\n"
    "v_permlane32_swap_b32 v45, v47\n"
    "v_pk_add_f32 v[44:45], v[44:45], v[46:47]\n"
    "v_cvt_pk_bf16_f32 v46, v44, v45\n"
    "v_cvt_pk_bf16_f32 v47, v44, v45\n"
    "v_cmp_u_f32 vcc, v44, v45\n"
    "s_nop 0\n"
    "v_permlane32_swap_b32 v46, v47\n"
    "s_cbranch_vccnz L_chkR%=\n"
    "L_okR%=:\n"
    "buffer_store_dwordx2 v[46:47], v12, s[36:39], s59 offen sc1\n"
    "s_branch L_ev2%=\n"
    "L_notask%=:\n"
    "s_add_i32 s2, s0, 1\n"
    "s_and_b32 s35, s2, 15\n"
    "s_mul_i32 s35, s35, 0xc000\n"
    "s_add_i32 s35, s35, s63\n"
    "s_and_b32 s75, s2, 7\n"
    "s_lshl_b32 s75, s75, 14\n"
    "s_add_i32 s75, s75, s62\n"
    "s_and_b32 s2, s2, 3\n"
    "s_mov_b32 s97, 0xfffd0000\n"
    "s_cmp_lg_u32 s2, 0\n"
    "s_cselect_b32 s97, 0x10000, s97\n"
    "s_cselect_b32 s2, 0, -1\n"
    "s_add_u32 s48, s48, s97\n"
    "s_addc_u32 s49, s49, s2\n"
    "s_add_u32 s50, s50, s97\n"
    "s_addc_u32 s51, s51, s2\n"
    "s_add_i32 s59, s59, s97\n"
    "s_waitcnt vmcnt(5)\n"
    "s_barrier\n"
    "v_mov_b32 v44, s0\n"
    "s_mov_b64 exec, s[46:47]\n"
    "buffer_store_dword v44, off, s[36:39], s65 sc1\n"
    "buffer_store_dword v20, off, s[36:39], s29 sc1\n"
    "s_mov_b64 exec, -1\n"
    "s_add_i32 m0, s64, 0\n"
    "s_add_i32 s2, s35, 0\n"
    "buffer_load_dwordx4 v0, s[4:7], s2 offen sc1 lds\n"
    "s_add_i32 m0, s64, 1024\n"
    "s_add_i32 s2, s35, 1024\n"
    "buffer_load_dwordx4 v0, s[4:7], s2 offen sc1 lds\n"
    "s_add_i32 m0, s64, 2048\n"
    "s_add_i32 s2, s35, 16384\n"
    "buffer_load_dwordx4 v0, s[4:7], s2 offen sc1 lds\n"
    "s_add_i32 m0, s64, 3072\n"
    "s_add_i32 s2, s35, 17408\n"
    "buffer_load_dwordx4 v0, s[4:7], s2 offen sc1 lds\n"
    "global_load_dwordx4 v[80:83], v0, s[54:55] offset:-4096\n"
    "global_load_dwordx4 v[84:87], v0, s[54:55] offset:-3072\n"
    "global_load_dwordx4 v[88:91], v0, s[54:55] offset:-2048\n"
    "global_load_dwordx4 v[92:95], v0, s[54:55] offset:-1024\n"
    "global_load_dwordx4 v[96:99], v0, s[54:55] offset:0\n"
    "L_ev2%=:\n"
    "s_mov_b32 s29, s44\n"
    "s_add_i32 s44, s44, s97\n"
    "s_sub_i32 s2, s34, s72\n"
    "s_sub_i32 s3, s96, s73\n"
    "s_mul_i32 s2, s2, s67\n"
    "v_cvt_f32_u32 v53, s3\n"
    "v_cvt_f32_u32 v54, s2\n"
    "v_rcp_f32 v53, v53\n"
    "s_and_b32 s2, s0, 3\n"
    "s_cmp_eq_u32 s2, 3\n"
    "v_mul_f32 v53, v53, v54\n"
    "v_cvt_u32_f32 v53, v53\n"
    "s_nop 0\n"
    "v_readfirstlane_b32 s2, v53\n"
    "s_cselect_b32 s72, s34, s72\n"
    "s_cselect_b32 s73, s96, s73\n"
    "s_cselect_b32 s45, s2, s45\n"
    "s_min_u32 s45, s45, 400\n"
    "s_add_i32 s3, s28, s74\n"
    "L_d5%=a:\n"
    "s_memrealtime s[78:79]\n"
    "s_waitcnt lgkmcnt(0)\n"
    "s_sub_i32 s2, s3, s78\n"
    "s_cmp_gt_i32 s2, 3\n"
    "s_cbranch_scc1 L_d5%=a\n"
    "L_d5%=:\n"
    "s_sub_i32 s2, s2, 1\n"
    "s_cmp_ge_i32 s2, 0\n"
    "s_cbranch_scc1 L_d5%=\n"
    "global_load_dwordx4 v[100:103], v0, s[54:55] offset:1024\n"
    "global_load_dwordx4 v[104:107], v0, s[54:55] offset:2048\n"
    "global_load_dwordx4 v[108:111], v0, s[54:55] offset:3072\n"
    "s_sub_i32 s98, s59, s97\n"
    "L_kv%=:\n"
    "s_mov_b32 exec_hi, 0\n"
    "v_add_u32 v3, s97, v3\n"
    "s_mov_b32 exec_hi, -1\n"
    "s_cmp_eq_u32 s6, 0\n"
    "s_cbranch_scc1 L_kvw%=\n"
    "L_kvd%=:\n"
    "s_add_i32 s0, s0, 1\n"
    "s_xor_b32 s1, s1, 0x90000\n"
    "s_add_i32 s3, s28, s68\n"
    "L_d2%=a:\n"
    "s_memrealtime s[78:79]\n"
    "s_waitcnt lgkmcnt(0)\n"
    "s_sub_i32 s2, s3, s78\n"
    "s_cmp_gt_i32 s2, 3\n"
    "s_cbranch_scc1 L_d2%=a\n"
    "L_d2%=:\n"
    "s_sub_i32 s2, s2, 1\n"
    "s_cmp_ge_i32 s2, 0\n"
    "s_cbranch_scc1 L_d2%=\n"
    "s_branch L_step%=\n"
    "L_fix0%=:\n"
    "v_add_u32 v13, 0x8000, v1\n"
    "global_load_dwordx4 v[28:31], v13, s[48:49] offset:-4096 sc1\n"
    "global_load_dwordx4 v[32:35], v13, s[48:49] offset:-3072 sc1\n"
    "global_load_dwordx4 v[36:39], v13, s[48:49] offset:-2048 sc1\n"
    "global_load_dwordx4 v[40:43], v13, s[48:49] offset:-1024 sc1\n"
    "global_load_dwordx4 v[44:47], v13, s[48:49] offset:0 sc1\n"
    "global_load_dwordx4 v[48:51], v13, s[48:49] offset:1024 sc1\n"
    "global_load_dwordx4 v[52:55], v13, s[48:49] offset:2048 sc1\n"
    "global_load_dwordx4 v[56:59], v13, s[48:49] offset:3072 sc1\n"
    "global_load_dwordx4 v[60:63], v13, s[50:51] offset:0 sc1\n"
    "s_memrealtime s[76:77]\n"
    "s_waitcnt vmcnt(8) lgkmcnt(0)\n"
    "v_cmp_eq_u32 vcc, -1, v31\n"
    "s_sub_i32 s2, s66, s76\n"
    "s_cbranch_vccz L_bk0%=\n"
    "s_cmp_lt_i32 s2, 0\n"
    "s_cbranch_scc0 L_fix0%=\n"
    "s_branch L_bk0%=\n"
    "L_fix1%=:\n"
    "v_add_u32 v13, 0x8000, v1\n"
    "global_load_dwordx4 v[32:35], v13, s[48:49] offset:-3072 sc1\n"
    "global_load_dwordx4 v[36:39], v13, s[48:49] offset:-2048 sc1\n"
    "global_load_dwordx4 v[40:43], v13, s[48:49] offset:-1024 sc1\n"
    "global_load_dwordx4 v[44:47], v13, s[48:49] offset:0 sc1\n"
    "global_load_dwordx4 v[48:51], v13, s[48:49] offset:1024 sc1\n"
    "global_load_dwordx4 v[52:55], v13, s[48:49] offset:2048 sc1\n"
    "global_load_dwordx4 v[56:59], v13, s[48:49] offset:3072 sc1\n"
    "global_load_dwordx4 v[60:63], v13, s[50:51] offset:0 sc1\n"
    "s_memrealtime s[76:77]\n"
    "s_waitcnt vmcnt(7) lgkmcnt(0)\n"
    "v_cmp_eq_u32 vcc, -1, v35\n"
    "s_sub_i32 s2, s66, s76\n"
    "s_cbranch_vccz L_bk1%=\n"
    "s_cmp_lt_i32 s2, 0\n"
    "s_cbranch_scc0 L_fix1%=\n"
    "s_branch L_bk1%=\n"
    "L_fix2%=:\n"
    "v_add_u32 v13, 0x8000, v1\n"
    "global_load_dwordx4 v[36:39], v13, s[48:49] offset:-2048 sc1\n"
    "global_load_dwordx4 v[40:43], v13, s[48:49] offset:-1024 sc1\n"
    "global_load_dwordx4 v[44:47], v13, s[48:49] offset:0 sc1\n"
    "global_load_dwordx4 v[48:51], v13, s[48:49] offset:1024 sc1\n"
    "global_load_dwordx4 v[52:55], v13, s[48:49] offset:2048 sc1\n"
    "global_load_dwordx4 v[56:59], v13, s[48:49] offset:3072 sc1\n"
    "global_load_dwordx4 v[60:63], v13, s[50:51] offset:0 sc1\n"
    "s_memrealtime s[76:77]\n"
    "s_waitcnt vmcnt(6) lgkmcnt(0)\n"
    "v_cmp_eq_u32 vcc, -1, v39\n"
    "s_sub_i32 s2, s66, s76\n"
    "s_cbranch_vccz L_bk2%=\n"
    "s_cmp_lt_i32 s2, 0\n"
    "s_cbranch_scc0 L_fix2%=\n"
    "s_branch L_bk2%=\n"
    "L_fix3%=:\n"
    "v_add_u32 v13, 0x8000, v1\n"
    "global_load_dwordx4 v[40:43], v13, s[48:49] offset:-1024 sc1\n"
    "global_load_dwordx4 v[44:47], v13, s[48:49] offset:0 sc1\n"
    "global_load_dwordx4 v[48:51], v13, s[48:49] offset:1024 sc1\n"
    "global_load_dwordx4 v[52:55], v13, s[48:49] offset:2048 sc1\n"
    "global_load_dwordx4 v[56:59], v13, s[48:49] offset:3072 sc1\n"
    "global_load_dwordx4 v[60:63], v13, s[50:51] offset:0 sc1\n"
    "s_memrealtime s[76:77]\n"
    "s_waitcnt vmcnt(5) lgkmcnt(0)\n"
    "v_cmp_eq_u32 vcc, -1, v43\n"
    "s_sub_i32 s2, s66, s76\n"
    "s_cbranch_vccz L_bk3%=\n"
    "s_cmp_lt_i32 s2, 0\n"
    "s_cbranch_scc0 L_fix3%=\n"
    "s_branch L_bk3%=\n"
    "L_fix4%=:\n"
    "v_add_u32 v13, 0x8000, v1\n"
    "global_load_dwordx4 v[44:47], v13, s[48:49] offset:0 sc1\n"
    "global_load_dwordx4 v[48:51], v13, s[48:49] offset:1024 sc1\n"
    "global_load_dwordx4 v[52:55], v13, s[48:49] offset:2048 sc1\n"
    "global_load_dwordx4 v[56:59], v13, s[48:49] offset:3072 sc1\n"
    "global_load_dwordx4 v[60:63], v13, s[50:51] offset:0 sc1\n"
    "s_memrealtime s[76:77]\n"
    "s_waitcnt vmcnt(4) lgkmcnt(0)\n"
    "v_cmp_eq_u32 vcc, -1, v47\n"
    "s_sub_i32 s2, s66, s76\n"
    "s_cbranch_vccz L_bk4%=\n"
    "s_cmp_lt_i32 s2, 0\n"
    "s_cbranch_scc0 L_fix4%=\n"
    "s_branch L_bk4%=\n"
    "L_fix5%=:\n"
    "v_add_u32 v13, 0x8000, v1\n"
    "global_load_dwordx4 v[48:51], v13, s[48:49] offset:1024 sc1\n"
    "global_load_dwordx4 v[52:55], v13, s[48:49] offset:2048 sc1\n"
    "global_load_dwordx4 v[56:59], v13, s[48:49] offset:3072 sc1\n"
    "global_load_dwordx4 v[60:63], v13, s[50:51] offset:0 sc1\n"
    "s_memrealtime s[76:77]\n"
    "s_waitcnt vmcnt(0) lgkmcnt(0)\n"
    "v_cmp_eq_u32 vcc, -1, v51\n"
    "s_sub_i32 s2, s66, s76\n"
    "s_cbranch_vccz L_bk5%=\n"
    "s_cmp_lt_i32 s2, 0\n"
    "s_cbranch_scc0 L_fix5%=\n"
    "s_branch L_bk5%=\n"
    "L_fix6%=:\n"
    "v_add_u32 v13, 0x8000, v1\n"
    "global_load_dwordx4 v[52:55], v13, s[48:49] offset:2048 sc1\n"
    "global_load_dwordx4 v[56:59], v13, s[48:49] offset:3072 sc1\n"
    "global_load_dwordx4 v[60:63], v13, s[50:51] offset:0 sc1\n"
    "s_memrealtime s[76:77]\n"
    "s_waitcnt vmcnt(0) lgkmcnt(0)\n"
    "v_cmp_eq_u32 vcc, -1, v55\n"
    "s_sub_i32 s2, s66, s76\n"
    "s_cbranch_vccz L_bk6%=\n"
    "s_cmp_lt_i32 s2, 0\n"
    "s_cbranch_scc0 L_fix6%=\n"
    "s_branch L_bk6%=\n"
    "L_fix7%=:\n"
    "v_add_u32 v13, 0x8000, v1\n"
    "global_load_dwordx4 v[56:59], v13, s[48:49] offset:3072 sc1\n"
    "global_load_dwordx4 v[60:63], v13, s[50:51] offset:0 sc1\n"
    "s_memrealtime s[76:77]\n"
    "s_waitcnt vmcnt(0) lgkmcnt(0)\n"
    "v_cmp_eq_u32 vcc, -1, v59\n"
    "s_sub_i32 s2, s66, s76\n"
    "s_cbranch_vccz L_bk7%=\n"
    "s_cmp_lt_i32 s2, 0\n"
    "s_cbranch_scc0 L_fix7%=\n"
    "s_branch L_bk7%=\n"
    "L_fix8%=:\n"
    "v_add_u32 v13, 0x8000, v1\n"
    "global_load_dwordx4 v[60:63], v13, s[50:51] offset:0 sc1\n"
    "s_memrealtime s[76:77]\n"
    "s_waitcnt vmcnt(0) lgkmcnt(0)\n"
    "v_cmp_eq_u32 vcc, -1, v63\n"
    "s_sub_i32 s2, s66, s76\n"
    "s_cbranch_vccz L_bk8%=\n"
    "s_cmp_lt_i32 s2, 0\n"
    "s_cbranch_scc0 L_fix8%=\n"
    "s_branch L_bk8%=\n"
    "L_chkA%=:\n"
    "v_max3_u32 v93, v28, v29, v30\n"
    "v_max3_u32 v93, v93, v31, v32\n"
    "v_max3_u32 v93, v93, v33, v34\n"
    "v_max3_u32 v93, v93, v35, v36\n"
    "v_max3_u32 v93, v93, v37, v38\n"
    "v_max3_u32 v93, v93, v39, v40\n"
    "v_max3_u32 v93, v93, v41, v42\n"
    "v_max3_u32 v93, v93, v43, v44\n"
    "v_max3_u32 v93, v93, v45, v46\n"
    "v_max3_u32 v93, v93, v47, v48\n"
    "v_max3_u32 v93, v93, v49, v50\n"
    "v_max3_u32 v93, v93, v51, v52\n"
    "v_max3_u32 v93, v93, v53, v54\n"
    "v_max3_u32 v93, v93, v55, v56\n"
    "v_max3_u32 v93, v93, v57, v58\n"
    "v_max3_u32 v93, v93, v59, v60\n"
    "v_max3_u32 v93, v93, v61, v62\n"
    "v_max_u32 v93, v93, v63\n"
    "v_cmp_eq_u32 vcc, -1, v93\n"
    "s_cbranch_vccz L_okA%=\n"
    "s_memrealtime s[76:77]\n"
    "s_waitcnt lgkmcnt(0)\n"
    "s_sub_i32 s2, s66, s76\n"
    "s_cmp_lt_i32 s2, 0\n"
    "s_cbranch_scc1 L_okA%=\n"
    "s_waitcnt vmcnt(0) lgkmcnt(0)\n"
    "s_nop 7\n"
    "s_branch L_redo%=\n"
    "L_chkR%=:\n"
    "v_max3_u32 v13, v28, v29, v30\n"
    "v_max3_u32 v13, v13, v31, v32\n"
    "v_max3_u32 v13, v13, v33, v34\n"
    "v_max3_u32 v13, v13, v35, v36\n"
    "v_max3_u32 v13, v13, v37, v38\n"
    "v_max3_u32 v13, v13, v39, v40\n"
    "v_max3_u32 v13, v13, v41, v42\n"
    "v_max_u32 v13, v13, v43\n"
    "v_cmp_eq_u32 vcc, -1, v13\n"
    "s_cbranch_vccz L_okR%=\n"
    "s_memrealtime s[76:77]\n"
    "s_waitcnt lgkmcnt(0)\n"
    "s_sub_i32 s2, s66, s76\n"
    "s_cmp_lt_i32 s2, 0\n"
    "s_cbranch_scc1 L_okR%=\n"
    "global_load_dwordx4 v[28:31], v0, s[98:99] offset:0 sc1\n"
    "global_load_dwordx4 v[32:35], v0, s[98:99] offset:1024 sc1\n"
    "global_load_dwordx4 v[36:39], v0, s[98:99] offset:2048 sc1\n"
    "global_load_dwordx4 v[40:43], v0, s[98:99] offset:3072 sc1\n"
    "s_waitcnt vmcnt(3)\n"
    "v_mfma_f32_16x16x32_bf16 v[44:47], v[28:31], v[48:51], 0\n"
    "s_waitcnt vmcnt(2)\n"
    "v_mfma_f32_16x16x32_bf16 v[44:47], v[32:35], v[48:51], v[44:47]\n"
    "s_waitcnt vmcnt(1)\n"
    "v_mfma_f32_16x16x32_bf16 v[44:47], v[36:39], v[48:51], v[44:47]\n"
    "s_waitcnt vmcnt(0)\n"
    "v_mfma_f32_16x16x32_bf16 v[44:47], v[40:43], v[48:51], v[44:47]\n"
    "s_nop 0\n"
    "s_branch L_red%=\n"
    "L_kvw%=:\n"
    "s_mov_b32 exec_lo, 0\n"
    "L_kvp%=:\n"
    "global_load_dword v13, v3, s[52:53] sc1\n"
    "s_waitcnt vmcnt(0)\n"
    "s_sub_i32 s2, s0, 8\n"
    "v_cmp_lt_i32 vcc, s0, v13\n"
    "v_cmp_lt_i32 s[76:77], s2, v13\n"
    "s_nop 4\n"
    "s_or_b32 s2, vcc_hi, 0xffff0000\n"
    "s_and_b32 s2, s2, s77\n"
    "s_cmp_eq_u32 s2, -1\n"
    "s_cbranch_scc1 L_kvx%=\n"
    "s_memrealtime s[76:77]\n"
    "s_waitcnt lgkmcnt(0)\n"
    "s_sub_i32 s2, s66, s76\n"
    "s_cmp_lt_i32 s2, 0\n"
    "s_cbranch_scc1 L_kvx%=\n"
    "s_sleep 2\n"
    "s_branch L_kvp%=\n"
    "L_kvx%=:\n"
    "s_mov_b32 exec_lo, -1\n"
    "s_add_i32 m0, s64, 0\n"
    "s_add_i32 s2, s35, 0\n"
    "buffer_load_dwordx4 v0, s[36:39], s2 offen sc1 lds\n"
    "s_add_i32 m0, s64, 1024\n"
    "s_add_i32 s2, s35, 1024\n"
    "buffer_load_dwordx4 v0, s[36:39], s2 offen sc1 lds\n"
    "s_add_i32 m0, s64, 2048\n"
    "s_add_i32 s2, s35, 16384\n"
    "buffer_load_dwordx4 v0, s[36:39], s2 offen sc1 lds\n"
    "s_add_i32 m0, s64, 3072\n"
    "s_add_i32 s2, s35, 17408\n"
    "buffer_load_dwordx4 v0, s[36:39], s2 offen sc1 lds\n"
    "s_branch L_kvd%=\n"
    "L_evt%=:\n"
    "global_load_dwordx4 v[80:83], v0, s[54:55] offset:-4096\n"
    "global_load_dwordx4 v[84:87], v0, s[54:55] offset:-3072\n"
    "global_load_dwordx4 v[88:91], v0, s[54:55] offset:-2048\n"
    "global_load_dwordx4 v[92:95], v0, s[54:55] offset:-1024\n"
    "global_load_dwordx4 v[96:99], v0, s[54:55] offset:0\n"
    "s_waitcnt vmcnt(12)\n"
    "v_mfma_f32_16x16x32_bf16 v[44:47], v[28:31], v[48:51], 0\n"
    "s_waitcnt vmcnt(11)\n"
    "v_mfma_f32_16x16x32_bf16 v[44:47], v[32:35], v[48:51], v[44:47]\n"
    "s_waitcnt vmcnt(10)\n"
    "v_mfma_f32_16x16x32_bf16 v[44:47], v[36:39], v[48:51], v[44:47]\n"
    "s_waitcnt vmcnt(9)\n"
    "v_mfma_f32_16x16x32_bf16 v[44:47], v[40:43], v[48:51], v[44:47]\n"
    "s_branch L_red%=\n"
    "L_tabw%=:\n"
    "s_memrealtime s[78:79]\n"
    "s_waitcnt lgkmcnt(0)\n"
    "s_sub_i32 s2, s66, s78\n"
    "s_cmp_lt_i32 s2, 0\n"
    "s_cbranch_scc1 L_tabd%=\n"
    "s_mov_b32 exec_hi, 0\n"
    "global_load_dword v13, v3, s[52:53] sc1\n"
    "s_mov_b32 exec_hi, -1\n"
    "s_waitcnt vmcnt(0)\n"
    "s_branch L_tab%=\n"
    "L_end%=:\n"
    "s_waitcnt vmcnt(0) lgkmcnt(0)\n"
    "s_barrier\n"
    "v_mov_b32 v44, 63\n"
    "s_and_b64 exec, s[46:47], 1\n"
    "buffer_store_dword v44, off, s[36:39], s65 sc1\n"
    "s_mov_b64 exec, -1\n"
    : "+{s[36:51]}"(s0), "+{s[52:67]}"(s1), "+{s[68:75]}"(s2), "+{v[0:7]}"(c0)
    : "{v[8:11]}"(c2), "{v12}"(c4), "{v[16:23]}"(c1), "{v[24:27]}"(c3),
      "{a[0:31]}"(wa[0]), "{a[32:63]}"(wa[1]), "{a[64:95]}"(wa[2]), "{a[96:127]}"(wa[3]), "{a[128:159]}"(wa[4]), "{a[160:191]}"(wa[5]), "{a[192:223]}"(wa[6]), "{a[224:255]}"(wa[7]),
      "{v[112:143]}"(wb[0]), "{v[144:175]}"(wb[1]), "{v[176:207]}"(wb[2]), "{v[208:239]}"(wb[3]), "{v[240:255]}"(wc)
    : "v13", "v28", "v29", "v30", "v31", "v32", "v33", "v34", "v35", "v36", "v37", "v38", "v39", "v40", "v41", "v42", "v43", "v44", "v45", "v46",
      "v47", "v48", "v49", "v50", "v51", "v52", "v53", "v54", "v55", "v56", "v57", "v58", "v59", "v60", "v61", "v62", "v63", "v64", "v65", "v66",
      "v67", "v68", "v69", "v70", "v71", "v72", "v73", "v74", "v75", "v76", "v77", "v78", "v79", "v80", "v81", "v82", "v83", "v84", "v85", "v86",
      "v87", "v88", "v89", "v90", "v91", "v92", "v93", "v94", "v95", "v96", "v97", "v98", "v99", "v100", "v101", "v102", "v103", "v104", "v105", "v106",
      "v107", "v108", "v109", "v110", "v111", "s0", "s1", "s2", "s3", "s4", "s5", "s6", "s7", "s28", "s29", "s30", "s31", "s34", "s35", "s76",
      "s77", "s78", "s79", "s96", "s97", "s98", "s99", "vcc", "scc", "memory");
  __builtin_amdgcn_endpgm();
}

DI int node_of_block(int blk, int rot) {
  const int x = (blk + rot) & 7, r = blk >> 3;
  if (x < 4) {
    if (r < 16) return N_CH + (r >> 3) * 32 + x * 8 + (r & 7);
    if (r < 20) return N_AT + ((r - 16) >> 1) * 8 + x * 2 + (r & 1);
    return N_GU + x * 12 + (r - 20);
  }
  if (r < 6) return N_WO + (x - 4) * 6 + r;
  if (r < 12) return N_DN + (x - 4) * 6 + (r - 6);
  if (r < 16) return N_RD + (x - 4) * 4 + (r - 12);
  return N_QKV + (x - 4) * 16 + (r - 16);
}

__global__ void __launch_bounds__(256, 1) rmt_geglu_kernel(Params p) {
  extern __shared__ __attribute__((aligned(16))) char smem[];
  LAS char* L = (LAS char*)smem;
  Ctx c;
  c.wsp = p.ws; c.ws = mk(p.ws, WS_USED); c.deadline = rtclk() + 5000000ull;
  c.lane = threadIdx.x & 63; c.w = rfl(threadIdx.x >> 6); c.par = 0;
  unsigned long long t_start = rtclk();
  int rot = (int)(__builtin_amdgcn_s_getreg(20 | (3 << 11)) & 7u);
  if (blockIdx.x == 0) st1((unsigned)rot, c.ws, threadIdx.x == 0 ? 0u : OOB, OFF_CTL + 256);
  else for (;;) { asm volatile("" ::: "memory"); rot = rfl(ld1(c.ws, 0u, OFF_CTL + 256)); if (rot >= 0) break; if (late(c)) { rot = 0; break; } __builtin_amdgcn_s_sleep(1); }
  const int node = node_of_block(blockIdx.x, rot);
  { const int x = (blockIdx.x + rot) & 7, r = blockIdx.x >> 3;
    if (TV(10, 1)) {
      if (x < 4) { if (r >= 8 && (r < 16 || r >= 20)) warm(p, L, c.lane, c.w, x, (r < 16 ? r - 8 : r - 12) * 4 + c.w, 80, 80, true); }
      else if (r < 12) warm(p, L, c.lane, c.w, x - 4, r * 4 + c.w, 48, 48, false);
    } }
  STAMPT(node, 135);
  if (node < N_QKV) chain_role(p, c, L, node);
  else if (node < N_AT) qkv_role(p, c, L, node - N_QKV);
  else if (node < N_WO) attn_role(p, c, L, node - N_AT);
  else if (node < N_GU) part_role<32>(p, c, L, node - N_WO);
  else if (node < N_DN) gu_role(p, c, L, node - N_GU);
  else if (node < N_RD) part_role<36>(p, c, L, node - N_DN);
  else red_role(p, c, node - N_RD);
}

extern "C" void kernel_launch(void* const* d_in, const int* in_sizes, int n_in, void* d_out, int out_size, void* d_ws, size_t ws_size,
                              hipStream_t stream) {
  (void)in_sizes; (void)n_in; (void)out_size; (void)ws_size;
  Params p;
  p.x = (const char*)d_in[0]; p.mem0 = (const char*)d_in[1]; p.wq = (const char*)d_in[2]; p.wk = (const char*)d_in[3];
  p.wv = (const char*)d_in[4]; p.wo = (const char*)d_in[5]; p.wg = (const char*)d_in[8]; p.wu = (const char*)d_in[9];
  p.wd = (const char*)d_in[10]; p.out = (char*)d_out; p.ws = (char*)d_ws;
  (void)hipFuncSetAttribute((const void*)rmt_geglu_kernel, hipFuncAttributeMaxDynamicSharedMemorySize, LDS_BYTES);
  (void)hipMemsetAsync(d_ws, 0xFF, CLEAR_BYTES, stream);
  hipLaunchKernelGGL(rmt_geglu_kernel, dim3(256), dim3(256), LDS_BYTES, stream, p);
}
```
